```python
import torch

def _softmax(x, axis=-1):
    return torch.softmax(x, dim=axis)
def _gelu(x):
    return torch.nn.functional.gelu(x, approximate="tanh")
def _silu(x):
    return torch.nn.functional.silu(x)
def _sigmoid(x):
    return torch.sigmoid(x)
def _layernorm(x, axis=-1, eps=1e-5):
    m = x.mean(dim=axis, keepdim=True)
    v = x.var(dim=axis, keepdim=True, unbiased=False)
    return (x - m) / torch.sqrt(v + eps)
def _scatter_add(tgt, idx, src, axis=0):
    return tgt.clone().index_add_(axis, idx, src)
def _segment_sum(src, seg, n):
    out = torch.zeros((n,) + src.shape[1:], dtype=src.dtype)
    return out.index_add_(0, seg, src)
def _pad(x, pw):
    flat = tuple(v for pair in reversed(pw) for v in pair)
    return torch.nn.functional.pad(x, flat)

kernel_name = 'sequential_preLN__sliding_block_cached__dense_activation_squared_relu__decode'

def setup_inputs():

    _w = (lambda a: (lambda t: t.to(torch.bfloat16) if t.is_floating_point() else t)(torch.as_tensor(a)))
    def _A_setup():
        B, Sq, D = 1, 1, 576
        Hq, Hkv, Sk, Dh = 32, 16, 65536, 128
        assert Hq % Hkv == 0, (Hq, Hkv)
        return {
            "x": _w((lambda *s: torch.randn(*s, dtype=torch.bfloat16) * 0.05)(B, Sq, D)),
            "k_cache": _w((lambda *s: torch.randn(*s, dtype=torch.bfloat16) * 3.2)(B, Hkv, Sk, Dh)),
            "v_cache": _w((lambda *s: torch.randn(*s, dtype=torch.bfloat16) * 0.4)(B, Hkv, Sk, Dh)),
            "wq": _w((lambda *s: torch.randn(*s, dtype=torch.bfloat16) * 3.2)(D, Hq * Dh) * (D ** -0.5)),
            "wo": _w((lambda *s: torch.randn(*s, dtype=torch.bfloat16) * 0.4)(Hq * Dh, D) * ((Hq * Dh) ** -0.5)),
        }
    _A_in = _A_setup()

    def _B_setup():
        B, S, D, F = 1, 1, 576, 4608
        return {
            "x": _w((lambda *s: torch.randn(*s, dtype=torch.bfloat16) * 0.05)(B, S, D)),
            "w1": _w((lambda *s: torch.randn(*s, dtype=torch.bfloat16) * 0.05)(D, F)),
            "b1": _w((lambda *s: torch.randn(*s, dtype=torch.bfloat16) * 0.8)(F)),
            "w2": _w((lambda *s: torch.randn(*s, dtype=torch.bfloat16) * 0.05)(F, D)),
            "b2": _w((lambda *s: torch.randn(*s, dtype=torch.bfloat16) * 3.2)(D)),
        }
    _B_in = _B_setup()

    return {
        "A_x": _A_in["x"],
        "A_k_cache": _A_in["k_cache"],
        "A_v_cache": _A_in["v_cache"],
        "A_wq": _A_in["wq"],
        "A_wo": _A_in["wo"],
        "B_x": _B_in["x"],
        "B_w1": _B_in["w1"],
        "B_b1": _B_in["b1"],
        "B_w2": _B_in["w2"],
        "B_b2": _B_in["b2"],
    }

def reference(A_x, A_k_cache, A_v_cache, A_wq, A_wo, B_x, B_w1, B_b1, B_w2, B_b2):
    def _A_ref(x, k_cache, v_cache, wq, wo):
        B, Sq, D = x.shape
        Hq, Dh = 32, 128
        Hkv, Sk = k_cache.shape[1], k_cache.shape[2]
        W = min(4096, max(256, Sk // 4))
        rep = Hq // Hkv
        q = (lambda x, *d: x.permute(*d))((x @ wq).reshape(B, Sq, Hq, Dh), 0, 2, 1, 3)
        k = (lambda x, n, axis: torch.repeat_interleave(x, n, dim=axis))(k_cache[:, :, Sk - W:, :], rep, axis=1)
        v = (lambda x, n, axis: torch.repeat_interleave(x, n, dim=axis))(v_cache[:, :, Sk - W:, :], rep, axis=1)
        scores = torch.einsum("bhqd,bhkd->bhqk", q, k) * (Dh ** -0.5)
        p = _softmax(scores, axis=-1)
        o = torch.einsum("bhqk,bhkd->bhqd", p, v)
        return (lambda x, *d: x.permute(*d))(o, 0, 2, 1, 3).reshape(B, Sq, Hq * Dh) @ wo

    def _B_ref(x, w1, b1, w2, b2):
        h = torch.einsum("bsd,df->bsf", x, w1) + b1
        zero = torch.zeros_like(h)
        r = torch.where(h > zero, h, zero)
        h = r * r
        out = torch.einsum("bsf,fd->bsd", h, w2) + b2
        return out

    x = A_x
    h = x + _A_ref(_layernorm(x, axis=-1), A_k_cache, A_v_cache, A_wq, A_wo)
    h2 = h + _B_ref(_layernorm(h, axis=-1), B_w1, B_b1, B_w2, B_b2)
    return h2
```

```cpp
#include <hip/hip_runtime.h>
#include <hip/hip_bf16.h>
#include <stdint.h>

typedef __attribute__((ext_vector_type(4))) unsigned int u32x4;
typedef __attribute__((ext_vector_type(2))) unsigned int u32x2;
typedef __attribute__((ext_vector_type(2))) __bf16 bf16x2_t;

namespace {

constexpr int D_MODEL = 576;
constexpr int SK = 65536;
constexpr int WIN = 4096;
constexpr int DH = 128;
constexpr int FF = 4608;
constexpr int NMLP = 144;

constexpr int EPOCH_OFF = 0;
constexpr int QG_OFF = 4096;
constexpr int AG_OFF = QG_OFF + 4 * 4096 * 8;
constexpr int WG_OFF = AG_OFF + 32 * 16 * 144 * 8;
constexpr int HG_OFF = WG_OFF + 256 * 576 * 8;
constexpr int MG_OFF = HG_OFF + 8192;
constexpr int WS_BYTES = MG_OFF + NMLP * 576 * 8;

constexpr int AUX_SC1 = 16;
constexpr unsigned SPIN_CAP = 200000u;

__device__ __forceinline__ float bf_lo(unsigned u) { return __uint_as_float(u << 16); }
__device__ __forceinline__ float bf_hi(unsigned u) { return __uint_as_float(u & 0xffff0000u); }
__device__ __forceinline__ float bf_us(unsigned short u) { return __uint_as_float(((unsigned)u) << 16); }
__device__ __forceinline__ float rbf(float f) {
  unsigned u = __float_as_uint(f);
  u += 0x7fffu + ((u >> 16) & 1u);
  return __uint_as_float(u & 0xffff0000u);
}
__device__ __forceinline__ unsigned short to_bf_us(float f) {
  unsigned u = __float_as_uint(f);
  u += 0x7fffu + ((u >> 16) & 1u);
  return (unsigned short)(u >> 16);
}
__device__ __forceinline__ float dot2(unsigned k, unsigned q, float acc) {
  return __builtin_amdgcn_fdot2_f32_bf16(__builtin_bit_cast(bf16x2_t, k), __builtin_bit_cast(bf16x2_t, q), acc, false);
}
#define BAR() asm volatile("s_waitcnt lgkmcnt(0)\n\ts_barrier" ::: "memory")
#define CFENCE() asm volatile("" ::: "memory")

__device__ __forceinline__ u32x4 ld_sc1(__amdgpu_buffer_rsrc_t rs, int off) {
  return __builtin_amdgcn_raw_buffer_load_b128(rs, off, 0, AUX_SC1);
}
__device__ __forceinline__ void st_sc1(__amdgpu_buffer_rsrc_t rs, int off, u32x4 v) {
  __builtin_amdgcn_raw_buffer_store_b128(v, rs, off, 0, AUX_SC1);
}
__device__ __forceinline__ void fma8(float (&acc)[8], float a, u32x4 w) {
  acc[0] += a * bf_lo(w.x); acc[1] += a * bf_hi(w.x);
  acc[2] += a * bf_lo(w.y); acc[3] += a * bf_hi(w.y);
  acc[4] += a * bf_lo(w.z); acc[5] += a * bf_hi(w.z);
  acc[6] += a * bf_lo(w.w); acc[7] += a * bf_hi(w.w);
}

}

struct Smem {
  float red[8][576];
  float att[8][2][132];
  float qred[9][64];
  unsigned qbf[128];
  float raw[16 * 16 + 32];
  float o16[16];
  float h[576];
  float w1red[9][32];
  unsigned tag;
};

template <bool HAS_KV, bool HAS_MLP>
__device__ __forceinline__ void data_body(Smem& sm, const int tid, const int lane, const int wave, const int c,
                                          const __amdgpu_buffer_rsrc_t rs,
                                          const unsigned short* __restrict__ x, const unsigned short* __restrict__ kc,
                                          const unsigned short* __restrict__ vc, const unsigned short* __restrict__ wq,
                                          const unsigned short* __restrict__ wo, const unsigned short* __restrict__ w1,
                                          const unsigned short* __restrict__ b1, const unsigned short* __restrict__ w2) {
    const int g = c >> 4, s = c & 15;
    const int cs = s & 3, kq = s >> 2;
    unsigned xd[5];
#pragma unroll
    for (int i = 0; i < 5; ++i) {
      const int idx = lane + 64 * i;
      xd[i] = ((const unsigned*)x)[idx < 288 ? idx : 287];
    }
    const int qrow0 = kq * 144 + (tid >> 3);
    const unsigned short xr0 = x[qrow0], xr1 = x[qrow0 + 72];
    u32x4 wqv[2];
    {
      const unsigned short* p = wq + (size_t)qrow0 * 4096 + g * 256 + cs * 64 + (tid & 7) * 8;
      wqv[0] = *(const u32x4*)p;
      wqv[1] = *(const u32x4*)(p + (size_t)72 * 4096);
    }
    u32x4 kv[8], vv[8];
    if constexpr (HAS_KV) {
      const size_t base = ((size_t)g * SK + (SK - WIN) + s * 256 + wave * 32 + (lane >> 4)) * DH + (lane & 15) * 8;
#pragma unroll
      for (int j = 0; j < 8; ++j) kv[j] = *(const u32x4*)(kc + base + (size_t)j * 4 * DH);
#pragma unroll
      for (int j = 0; j < 8; ++j) vv[j] = *(const u32x4*)(vc + base + (size_t)j * 4 * DH);
    }
    u32x4 wov[2];
    {
      const unsigned short* p = wo + (size_t)c * 16 * 576 + tid * 8;
      wov[0] = *(const u32x4*)p;
      wov[1] = *(const u32x4*)(p + 576 * 8);
    }
    u32x4 w1v[4], w2v[4];
    unsigned short b1r[4];
    if constexpr (HAS_MLP) {
      const unsigned short* p1 = w1 + (size_t)(tid >> 2) * FF + c * 32 + (tid & 3) * 8;
      const unsigned short* p2 = w2 + (size_t)c * 32 * 576 + tid * 8;
#pragma unroll
      for (int i = 0; i < 4; ++i) w1v[i] = *(const u32x4*)(p1 + (size_t)i * 144 * FF);
#pragma unroll
      for (int i = 0; i < 4; ++i) w2v[i] = *(const u32x4*)(p2 + (size_t)i * 576 * 8);
#pragma unroll
      for (int i = 0; i < 4; ++i) b1r[i] = b1[c * 32 + tid / 72 + 8 * i];
    }
    CFENCE();
    __builtin_amdgcn_sched_barrier(0);

    float xn0, xn1;
    {
      float sum = 0.f;
#pragma unroll
      for (int i = 0; i < 5; ++i) sum += (lane + 64 * i < 288) ? (bf_lo(xd[i]) + bf_hi(xd[i])) : 0.f;
#pragma unroll
      for (int o = 1; o < 64; o <<= 1) sum += __shfl_xor(sum, o);
      const float mean = sum * (1.f / 576.f);
      float sq = 0.f;
#pragma unroll
      for (int i = 0; i < 5; ++i) {
        if (lane + 64 * i < 288) {
          const float a = bf_lo(xd[i]) - mean, b = bf_hi(xd[i]) - mean;
          sq += a * a + b * b;
        }
      }
#pragma unroll
      for (int o = 1; o < 64; o <<= 1) sq += __shfl_xor(sq, o);
      const float mb = rbf(mean);
      const float sd = rbf(sqrtf(rbf(rbf(sq * (1.f / 576.f)) + 1e-5f)));
      xn0 = rbf(rbf(bf_us(xr0) - mb) / sd);
      xn1 = rbf(rbf(bf_us(xr1) - mb) / sd);
    }
    {
      float acc[8];
#pragma unroll
      for (int k = 0; k < 8; ++k) acc[k] = 0.f;
      fma8(acc, xn0, wqv[0]);
      fma8(acc, xn1, wqv[1]);
#pragma unroll
      for (int o = 8; o < 64; o <<= 1)
#pragma unroll
        for (int k = 0; k < 8; ++k) acc[k] += __shfl_xor(acc[k], o);
      if (lane < 8) {
#pragma unroll
        for (int k = 0; k < 8; ++k) sm.qred[wave][lane * 8 + k] = acc[k];
      }
    }
    BAR();
    const unsigned tag = sm.tag;
    if (wave == 0) {
      float v = 0.f;
#pragma unroll
      for (int w = 0; w < 9; ++w) v += sm.qred[w][lane];
      u32x2 gv; gv.x = __float_as_uint(v); gv.y = tag;
      __builtin_amdgcn_raw_buffer_store_b64(gv, rs, QG_OFF + (kq * 4096 + g * 256 + cs * 64 + lane) * 8, 0, AUX_SC1);
    }
    BAR();
    if constexpr (HAS_KV) {
      unsigned qa[4], qb[4];
#pragma unroll
      for (int k = 0; k < 4; ++k) { qa[k] = sm.qbf[(lane & 15) * 4 + k]; qb[k] = sm.qbf[64 + (lane & 15) * 4 + k]; }
      float sa[8], sb[8];
#pragma unroll
      for (int j = 0; j < 8; ++j) {
        float a = dot2(kv[j].x, qa[0], 0.f), b = dot2(kv[j].x, qb[0], 0.f);
        a = dot2(kv[j].y, qa[1], a); b = dot2(kv[j].y, qb[1], b);
        a = dot2(kv[j].z, qa[2], a); b = dot2(kv[j].z, qb[2], b);
        a = dot2(kv[j].w, qa[3], a); b = dot2(kv[j].w, qb[3], b);
        sa[j] = a; sb[j] = b;
      }
#pragma unroll
      for (int o = 1; o < 16; o <<= 1)
#pragma unroll
        for (int j = 0; j < 8; ++j) { sa[j] += __shfl_xor(sa[j], o); sb[j] += __shfl_xor(sb[j], o); }
      float ma = -3.0e38f, mb = -3.0e38f;
#pragma unroll
      for (int j = 0; j < 8; ++j) {
        sa[j] = rbf(rbf(sa[j]) * 0.08838834764831845f);
        sb[j] = rbf(rbf(sb[j]) * 0.08838834764831845f);
        ma = fmaxf(ma, sa[j]); mb = fmaxf(mb, sb[j]);
      }
#pragma unroll
      for (int o = 16; o < 64; o <<= 1) { ma = fmaxf(ma, __shfl_xor(ma, o)); mb = fmaxf(mb, __shfl_xor(mb, o)); }
      float la = 0.f, lb = 0.f;
      float oa[8], ob[8];
#pragma unroll
      for (int k = 0; k < 8; ++k) { oa[k] = 0.f; ob[k] = 0.f; }
#pragma unroll
      for (int j = 0; j < 8; ++j) {
        const float pa = __expf(sa[j] - ma), pb = __expf(sb[j] - mb);
        la += pa; lb += pb;
        fma8(oa, pa, vv[j]);
        fma8(ob, pb, vv[j]);
      }
#pragma unroll
      for (int o = 16; o < 64; o <<= 1) {
        la += __shfl_xor(la, o); lb += __shfl_xor(lb, o);
#pragma unroll
        for (int k = 0; k < 8; ++k) { oa[k] += __shfl_xor(oa[k], o); ob[k] += __shfl_xor(ob[k], o); }
      }
      if (lane < 16) {
#pragma unroll
        for (int k = 0; k < 8; ++k) { sm.att[wave][0][4 + lane * 8 + k] = oa[k]; sm.att[wave][1][4 + lane * 8 + k] = ob[k]; }
        if (lane == 0) { sm.att[wave][0][0] = ma; sm.att[wave][0][1] = la; sm.att[wave][1][0] = mb; sm.att[wave][1][1] = lb; }
      }
    }
    BAR();
    if (tid < 128) {
      const int h = tid >> 6, d2 = (tid & 63) * 2;
      float M = sm.att[0][h][0];
#pragma unroll
      for (int w = 1; w < 8; ++w) M = fmaxf(M, sm.att[w][h][0]);
      float L = 0.f, o0 = 0.f, o1 = 0.f;
#pragma unroll
      for (int w = 0; w < 8; ++w) {
        const float e = __expf(sm.att[w][h][0] - M);
        L += e * sm.att[w][h][1];
        o0 += e * sm.att[w][h][4 + d2];
        o1 += e * sm.att[w][h][5 + d2];
      }
      const int blk = AG_OFF + ((2 * g + h) * 16 + s) * 144 * 8;
      u32x4 gv; gv.x = __float_as_uint(o0); gv.y = tag; gv.z = __float_as_uint(o1); gv.w = tag;
      st_sc1(rs, blk + (16 + d2) * 8, gv);
      if ((tid & 63) == 0) {
        u32x4 mv; mv.x = __float_as_uint(M); mv.y = tag; mv.z = __float_as_uint(L); mv.w = tag;
        st_sc1(rs, blk, mv);
      }
    }
    BAR();
    {
      const int r0 = tid / 72, cc = tid - r0 * 72;
      float acc[8];
#pragma unroll
      for (int k = 0; k < 8; ++k) acc[k] = 0.f;
      fma8(acc, sm.o16[r0], wov[0]);
      fma8(acc, sm.o16[r0 + 8], wov[1]);
      *(float4*)&sm.red[r0][cc * 8] = make_float4(acc[0], acc[1], acc[2], acc[3]);
      *(float4*)&sm.red[r0][cc * 8 + 4] = make_float4(acc[4], acc[5], acc[6], acc[7]);
    }
    BAR();
    if (tid < 288) {
      float v0 = 0.f, v1 = 0.f;
#pragma unroll
      for (int r = 0; r < 8; ++r) { v0 += sm.red[r][2 * tid]; v1 += sm.red[r][2 * tid + 1]; }
      u32x4 gv; gv.x = __float_as_uint(v0); gv.y = tag; gv.z = __float_as_uint(v1); gv.w = tag;
      st_sc1(rs, WG_OFF + (c * 576 + 2 * tid) * 8, gv);
    }
    if constexpr (HAS_MLP) {
      BAR();
      float xn[4];
      {
        float hv[9];
        float sum = 0.f;
#pragma unroll
        for (int i = 0; i < 9; ++i) { hv[i] = sm.h[lane + 64 * i]; sum += hv[i]; }
#pragma unroll
        for (int o = 1; o < 64; o <<= 1) sum += __shfl_xor(sum, o);
        const float mean = sum * (1.f / 576.f);
        float sq = 0.f;
#pragma unroll
        for (int i = 0; i < 9; ++i) { const float a = hv[i] - mean; sq += a * a; }
#pragma unroll
        for (int o = 1; o < 64; o <<= 1) sq += __shfl_xor(sq, o);
        const float mb = rbf(mean);
        const float sd = rbf(sqrtf(rbf(rbf(sq * (1.f / 576.f)) + 1e-5f)));
#pragma unroll
        for (int i = 0; i < 4; ++i) xn[i] = rbf(rbf(sm.h[(tid >> 2) + 144 * i] - mb) / sd);
      }
      {
        float acc[8];
#pragma unroll
        for (int k = 0; k < 8; ++k) acc[k] = 0.f;
#pragma unroll
        for (int i = 0; i < 4; ++i) fma8(acc, xn[i], w1v[i]);
#pragma unroll
        for (int o = 4; o < 64; o <<= 1)
#pragma unroll
          for (int k = 0; k < 8; ++k) acc[k] += __shfl_xor(acc[k], o);
        if (lane < 4) {
#pragma unroll
          for (int k = 0; k < 8; ++k) sm.w1red[wave][lane * 8 + k] = acc[k];
        }
      }
      BAR();
      {
        const int r0 = tid / 72, cc = tid - r0 * 72;
        float acc[8];
#pragma unroll
        for (int k = 0; k < 8; ++k) acc[k] = 0.f;
#pragma unroll
        for (int i = 0; i < 4; ++i) {
          float v = 0.f;
#pragma unroll
          for (int w = 0; w < 9; ++w) v += sm.w1red[w][r0 + 8 * i];
          v = rbf(rbf(v) + bf_us(b1r[i]));
          v = fmaxf(v, 0.f);
          v = rbf(v * v);
          fma8(acc, v, w2v[i]);
        }
        *(float4*)&sm.red[r0][cc * 8] = make_float4(acc[0], acc[1], acc[2], acc[3]);
        *(float4*)&sm.red[r0][cc * 8 + 4] = make_float4(acc[4], acc[5], acc[6], acc[7]);
      }
      BAR();
      if (tid < 288) {
        float v0 = 0.f, v1 = 0.f;
#pragma unroll
        for (int r = 0; r < 8; ++r) { v0 += sm.red[r][2 * tid]; v1 += sm.red[r][2 * tid + 1]; }
        u32x4 gv; gv.x = __float_as_uint(v0); gv.y = tag; gv.z = __float_as_uint(v1); gv.w = tag;
        st_sc1(rs, MG_OFF + (c * 576 + 2 * tid) * 8, gv);
      }
    }
}

__global__ void __launch_bounds__(640, 3)
fused_decode_block(const unsigned short* __restrict__ x, const unsigned short* __restrict__ kc,
                   const unsigned short* __restrict__ vc, const unsigned short* __restrict__ wq,
                   const unsigned short* __restrict__ wo, const unsigned short* __restrict__ w1,
                   const unsigned short* __restrict__ b1, const unsigned short* __restrict__ w2,
                   const unsigned short* __restrict__ b2, unsigned short* __restrict__ out, char* ws) {
  __shared__ __attribute__((aligned(16))) Smem sm;
  const int tid = threadIdx.x;
  const int lane = tid & 63;
  const int wave = __builtin_amdgcn_readfirstlane(tid >> 6);
  const int c = blockIdx.x;
  const int g = c >> 4;
  const __amdgpu_buffer_rsrc_t rs = __builtin_amdgcn_make_buffer_rsrc((void*)ws, 0, WS_BYTES, 0x00020000);

  if (wave < 8) {
    if (c < NMLP) data_body<true, true>(sm, tid, lane, wave, c, rs, x, kc, vc, wq, wo, w1, b1, w2);
    else data_body<true, false>(sm, tid, lane, wave, c, rs, x, kc, vc, wq, wo, w1, b1, w2);
  } else if (wave == 8) {
    if (c < NMLP) data_body<false, true>(sm, tid, lane, wave, c, rs, x, kc, vc, wq, wo, w1, b1, w2);
    else data_body<false, false>(sm, tid, lane, wave, c, rs, x, kc, vc, wq, wo, w1, b1, w2);
  } else {
    const unsigned tag = __builtin_amdgcn_raw_buffer_load_b32(rs, EPOCH_OFF, 0, AUX_SC1) + 1u;
    float xc[4], b2c[4];
    if (c < NMLP) {
#pragma unroll
      for (int k = 0; k < 4; ++k) { xc[k] = bf_us(x[4 * c + k]); b2c[k] = bf_us(b2[4 * c + k]); }
    } else {
#pragma unroll
      for (int k = 0; k < 4; ++k) { xc[k] = 0.f; b2c[k] = 0.f; }
    }
    if (lane == 0) sm.tag = tag;
    BAR();
    {
      float q4[4];
      for (unsigned spin = 0; spin < SPIN_CAP; ++spin) {
        CFENCE();
        u32x4 a[4], b[4];
#pragma unroll
        for (int k = 0; k < 4; ++k) {
          const int off = QG_OFF + (k * 4096 + g * 256 + lane * 4) * 8;
          a[k] = ld_sc1(rs, off);
          b[k] = ld_sc1(rs, off + 16);
        }
        bool ok = true;
#pragma unroll
        for (int k = 0; k < 4; ++k) q4[k] = 0.f;
#pragma unroll
        for (int k = 0; k < 4; ++k) {
          ok &= (a[k].y == tag) & (a[k].w == tag) & (b[k].y == tag) & (b[k].w == tag);
          q4[0] += __uint_as_float(a[k].x); q4[1] += __uint_as_float(a[k].z);
          q4[2] += __uint_as_float(b[k].x); q4[3] += __uint_as_float(b[k].z);
        }
        if (__all(ok)) break;
        __builtin_amdgcn_s_sleep(1);
      }
      sm.qbf[lane * 2] = (unsigned)to_bf_us(q4[0]) | ((unsigned)to_bf_us(q4[1]) << 16);
      sm.qbf[lane * 2 + 1] = (unsigned)to_bf_us(q4[2]) | ((unsigned)to_bf_us(q4[3]) << 16);
    }
    BAR();
    BAR();
    {
      const int hblk = AG_OFF + (c >> 3) * 16 * 144 * 8;
      const int j16 = (c & 7) * 16;
      for (unsigned spin = 0; spin < SPIN_CAP; ++spin) {
        CFENCE();
        u32x4 ml = ld_sc1(rs, hblk + (lane & 15) * 144 * 8);
        u32x4 o0 = ld_sc1(rs, hblk + ((lane >> 3) * 144 + 16 + j16 + (lane & 7) * 2) * 8);
        u32x4 o1 = ld_sc1(rs, hblk + ((8 + (lane >> 3)) * 144 + 16 + j16 + (lane & 7) * 2) * 8);
        const bool ok = (ml.y == tag) & (ml.w == tag) & (o0.y == tag) & (o0.w == tag) & (o1.y == tag) & (o1.w == tag);
        if (lane < 16) { sm.raw[256 + lane * 2] = __uint_as_float(ml.x); sm.raw[257 + lane * 2] = __uint_as_float(ml.z); }
        sm.raw[(lane >> 3) * 16 + (lane & 7) * 2] = __uint_as_float(o0.x);
        sm.raw[(lane >> 3) * 16 + (lane & 7) * 2 + 1] = __uint_as_float(o0.z);
        sm.raw[(8 + (lane >> 3)) * 16 + (lane & 7) * 2] = __uint_as_float(o1.x);
        sm.raw[(8 + (lane >> 3)) * 16 + (lane & 7) * 2 + 1] = __uint_as_float(o1.z);
        if (__all(ok)) break;
        __builtin_amdgcn_s_sleep(1);
      }
      asm volatile("s_waitcnt lgkmcnt(0)" ::: "memory");
      if (lane < 16) {
        float M = sm.raw[256];
#pragma unroll
        for (int sp = 1; sp < 16; ++sp) M = fmaxf(M, sm.raw[256 + sp * 2]);
        float L = 0.f, o = 0.f;
#pragma unroll
        for (int sp = 0; sp < 16; ++sp) {
          const float e = __expf(sm.raw[256 + sp * 2] - M);
          L += e * sm.raw[257 + sp * 2];
          o += e * sm.raw[sp * 16 + lane];
        }
        sm.o16[lane] = rbf(o / L);
      }
    }
    BAR();
    BAR();
    if (c < NMLP) {
      float h4[4];
      {
        float v[4];
        for (unsigned spin = 0; spin < SPIN_CAP; ++spin) {
          CFENCE();
          u32x4 a[4], b[4];
#pragma unroll
          for (int i = 0; i < 4; ++i) {
            const int off = WG_OFF + ((lane + 64 * i) * 576 + 4 * c) * 8;
            a[i] = ld_sc1(rs, off);
            b[i] = ld_sc1(rs, off + 16);
          }
          bool ok = true;
#pragma unroll
          for (int k = 0; k < 4; ++k) v[k] = 0.f;
#pragma unroll
          for (int i = 0; i < 4; ++i) {
            ok &= (a[i].y == tag) & (a[i].w == tag) & (b[i].y == tag) & (b[i].w == tag);
            v[0] += __uint_as_float(a[i].x); v[1] += __uint_as_float(a[i].z);
            v[2] += __uint_as_float(b[i].x); v[3] += __uint_as_float(b[i].z);
          }
          if (__all(ok)) break;
          __builtin_amdgcn_s_sleep(1);
        }
#pragma unroll
        for (int o = 1; o < 64; o <<= 1)
#pragma unroll
          for (int k = 0; k < 4; ++k) v[k] += __shfl_xor(v[k], o);
#pragma unroll
        for (int k = 0; k < 4; ++k) h4[k] = rbf(xc[k] + rbf(v[k]));
        if (lane == 0) {
          u32x4 g0, g1;
          g0.x = __float_as_uint(h4[0]); g0.y = tag; g0.z = __float_as_uint(h4[1]); g0.w = tag;
          g1.x = __float_as_uint(h4[2]); g1.y = tag; g1.z = __float_as_uint(h4[3]); g1.w = tag;
          st_sc1(rs, HG_OFF + 4 * c * 8, g0);
          st_sc1(rs, HG_OFF + 4 * c * 8 + 16, g1);
        }
      }
      {
        for (unsigned spin = 0; spin < SPIN_CAP; ++spin) {
          CFENCE();
          u32x4 a[5];
#pragma unroll
          for (int i = 0; i < 5; ++i) {
            const int id = lane + 64 * i;
            if (id < 288) a[i] = ld_sc1(rs, HG_OFF + id * 16);
            else { a[i].x = 0u; a[i].y = tag; a[i].z = 0u; a[i].w = tag; }
          }
          bool ok = true;
#pragma unroll
          for (int i = 0; i < 5; ++i) {
            const int id = lane + 64 * i;
            ok &= (a[i].y == tag) & (a[i].w == tag);
            if (id < 288) { sm.h[2 * id] = __uint_as_float(a[i].x); sm.h[2 * id + 1] = __uint_as_float(a[i].z); }
          }
          if (__all(ok)) break;
          __builtin_amdgcn_s_sleep(1);
        }
      }
      BAR();
      BAR();
      BAR();
      {
        float v[4];
        for (unsigned spin = 0; spin < SPIN_CAP; ++spin) {
          CFENCE();
          u32x4 a[3], b[3];
#pragma unroll
          for (int i = 0; i < 3; ++i) {
            const int sl = lane + 64 * i;
            if (sl < NMLP) {
              const int off = MG_OFF + (sl * 576 + 4 * c) * 8;
              a[i] = ld_sc1(rs, off);
              b[i] = ld_sc1(rs, off + 16);
            } else {
              a[i].x = 0u; a[i].y = tag; a[i].z = 0u; a[i].w = tag;
              b[i] = a[i];
            }
          }
          bool ok = true;
#pragma unroll
          for (int k = 0; k < 4; ++k) v[k] = 0.f;
#pragma unroll
          for (int i = 0; i < 3; ++i) {
            ok &= (a[i].y == tag) & (a[i].w == tag) & (b[i].y == tag) & (b[i].w == tag);
            v[0] += __uint_as_float(a[i].x); v[1] += __uint_as_float(a[i].z);
            v[2] += __uint_as_float(b[i].x); v[3] += __uint_as_float(b[i].z);
          }
          if (__all(ok)) break;
          __builtin_amdgcn_s_sleep(1);
        }
#pragma unroll
        for (int o = 1; o < 64; o <<= 1)
#pragma unroll
          for (int k = 0; k < 4; ++k) v[k] += __shfl_xor(v[k], o);
        if (lane == 0) {
          u32x2 ov;
          ov.x = (unsigned)to_bf_us(h4[0] + rbf(rbf(v[0]) + b2c[0])) | ((unsigned)to_bf_us(h4[1] + rbf(rbf(v[1]) + b2c[1])) << 16);
          ov.y = (unsigned)to_bf_us(h4[2] + rbf(rbf(v[2]) + b2c[2])) | ((unsigned)to_bf_us(h4[3] + rbf(rbf(v[3]) + b2c[3])) << 16);
          *(u32x2*)(out + 4 * c) = ov;
        }
        if (c == 0 && lane == 0) __builtin_amdgcn_raw_buffer_store_b32(tag, rs, EPOCH_OFF, 0, AUX_SC1);
      }
    }
  }
}

extern "C" void kernel_launch(void* const* d_in, const int* in_sizes, int n_in,
                              void* d_out, int out_size, void* d_ws, size_t ws_size,
                              hipStream_t stream) {
  (void)in_sizes; (void)n_in; (void)out_size; (void)ws_size;
  fused_decode_block<<<256, 640, 0, stream>>>(
      (const unsigned short*)d_in[0], (const unsigned short*)d_in[1], (const unsigned short*)d_in[2],
      (const unsigned short*)d_in[3], (const unsigned short*)d_in[4], (const unsigned short*)d_in[6],
      (const unsigned short*)d_in[7], (const unsigned short*)d_in[8], (const unsigned short*)d_in[9],
      (unsigned short*)d_out, (char*)d_ws);
}
```

```cpp
#include <hip/hip_runtime.h>
#include <hip/hip_bf16.h>
#include <stdint.h>

typedef __attribute__((ext_vector_type(4))) unsigned int u32x4;
typedef __attribute__((ext_vector_type(2))) unsigned int u32x2;
typedef __attribute__((ext_vector_type(2))) __bf16 bf16x2_t;

namespace {

constexpr int SK = 65536;
constexpr int WIN = 4096;
constexpr int DH = 128;
constexpr int FF = 4608;
constexpr int NMLP = 144;

constexpr int EPOCH_OFF = 0;
constexpr int QG_OFF = 4096;
constexpr int AG_OFF = QG_OFF + 4 * 4096 * 8;
constexpr int WG_OFF = AG_OFF + 32 * 16 * 144 * 8;
constexpr int HG_OFF = WG_OFF + 72 * 256 * 64;
constexpr int MG_OFF = HG_OFF + 8192;
constexpr int WS_BYTES = MG_OFF + 144 * 144 * 32;

constexpr int AUX_SC1 = 16;
constexpr unsigned SPIN_CAP = 200000u;

__device__ __forceinline__ float bf_lo(unsigned u) { return __uint_as_float(u << 16); }
__device__ __forceinline__ float bf_hi(unsigned u) { return __uint_as_float(u & 0xffff0000u); }
__device__ __forceinline__ float bf_us(unsigned short u) { return __uint_as_float(((unsigned)u) << 16); }
__device__ __forceinline__ float rbf(float f) {
  unsigned u = __float_as_uint(f);
  u += 0x7fffu + ((u >> 16) & 1u);
  return __uint_as_float(u & 0xffff0000u);
}
__device__ __forceinline__ unsigned short to_bf_us(float f) {
  unsigned u = __float_as_uint(f);
  u += 0x7fffu + ((u >> 16) & 1u);
  return (unsigned short)(u >> 16);
}
__device__ __forceinline__ float dot2(unsigned k, unsigned q, float acc) {
  return __builtin_amdgcn_fdot2_f32_bf16(__builtin_bit_cast(bf16x2_t, k), __builtin_bit_cast(bf16x2_t, q), acc, false);
}
#define BAR() asm volatile("s_waitcnt lgkmcnt(0)\n\ts_barrier" ::: "memory")
#define RAWBAR() asm volatile("s_barrier" ::: "memory")
#define CFENCE() asm volatile("" ::: "memory")
#define SCHED() __builtin_amdgcn_sched_barrier(0)
#define STAMP(i) do { } while (0)
#define DSTAMP(i) do { } while (0)
#define CSTAMP(i) do { } while (0)

template <int CTRL>
__device__ __forceinline__ float dppf(float x) {
  return __int_as_float(__builtin_amdgcn_update_dpp(0, __float_as_int(x), CTRL, 0xf, 0xf, true));
}
constexpr int DPP_X1 = 0xB1, DPP_X2 = 0x4E, DPP_ROR4 = 0x124, DPP_ROR8 = 0x128;
__device__ __forceinline__ float sum_x16(float x) {
  auto r = __builtin_amdgcn_permlane16_swap(__float_as_uint(x), __float_as_uint(x), false, false);
  return __uint_as_float(r[0]) + __uint_as_float(r[1]);
}
__device__ __forceinline__ float sum_x32(float x) {
  auto r = __builtin_amdgcn_permlane32_swap(__float_as_uint(x), __float_as_uint(x), false, false);
  return __uint_as_float(r[0]) + __uint_as_float(r[1]);
}
__device__ __forceinline__ float max_x16(float x) {
  auto r = __builtin_amdgcn_permlane16_swap(__float_as_uint(x), __float_as_uint(x), false, false);
  return fmaxf(__uint_as_float(r[0]), __uint_as_float(r[1]));
}
__device__ __forceinline__ float max_x32(float x) {
  auto r = __builtin_amdgcn_permlane32_swap(__float_as_uint(x), __float_as_uint(x), false, false);
  return fmaxf(__uint_as_float(r[0]), __uint_as_float(r[1]));
}
__device__ __forceinline__ float sum_row16(float x) {
  x += dppf<DPP_X1>(x); x += dppf<DPP_X2>(x); x += dppf<DPP_ROR4>(x); x += dppf<DPP_ROR8>(x);
  return x;
}
__device__ __forceinline__ float sum_wave(float x) { return sum_x32(sum_x16(sum_row16(x))); }

__device__ __forceinline__ u32x4 ld_nt(const unsigned short* p) { return __builtin_nontemporal_load((const u32x4*)p); }
__device__ __forceinline__ u32x4 ld_sc1(__amdgpu_buffer_rsrc_t rs, int off) {
  return __builtin_amdgcn_raw_buffer_load_b128(rs, off, 0, AUX_SC1);
}
__device__ __forceinline__ void st_sc1(__amdgpu_buffer_rsrc_t rs, int off, u32x4 v) {
  __builtin_amdgcn_raw_buffer_store_b128(v, rs, off, 0, AUX_SC1);
}
__device__ __forceinline__ void fma8(float (&acc)[8], float a, u32x4 w) {
  acc[0] += a * bf_lo(w.x); acc[1] += a * bf_hi(w.x);
  acc[2] += a * bf_lo(w.y); acc[3] += a * bf_hi(w.y);
  acc[4] += a * bf_lo(w.z); acc[5] += a * bf_hi(w.z);
  acc[6] += a * bf_lo(w.w); acc[7] += a * bf_hi(w.w);
}

template <int N>
__device__ __forceinline__ void gather(const __amdgpu_buffer_rsrc_t rs, const int (&off)[N], const unsigned tag,
                                       u32x4 (&res)[N]) {
  u32x4 a[N], b[N];
#pragma unroll
  for (int k = 0; k < N; ++k) a[k] = ld_sc1(rs, off[k]);
  for (unsigned spin = 0; spin < SPIN_CAP; ++spin) {
    CFENCE();
#pragma unroll
    for (int k = 0; k < N; ++k) b[k] = ld_sc1(rs, off[k]);
    bool ok = true;
#pragma unroll
    for (int k = 0; k < N; ++k) ok &= (a[k].y == tag) & (a[k].w == tag);
    if (__all(ok)) {
#pragma unroll
      for (int k = 0; k < N; ++k) res[k] = a[k];
      return;
    }
    CFENCE();
#pragma unroll
    for (int k = 0; k < N; ++k) a[k] = ld_sc1(rs, off[k]);
    ok = true;
#pragma unroll
    for (int k = 0; k < N; ++k) ok &= (b[k].y == tag) & (b[k].w == tag);
    if (__all(ok)) {
#pragma unroll
      for (int k = 0; k < N; ++k) res[k] = b[k];
      return;
    }
  }
#pragma unroll
  for (int k = 0; k < N; ++k) res[k] = a[k];
}

struct Smem {
  float red[8][576];
  float att[8][2][132];
  float xn[576];
  unsigned qbf[128];
  float raw[16 * 16 + 32];
  float o16[16];
  float w1red[9][32];
  float h[576];
  float part[8][8];
  unsigned tag;
};

template <bool HAS_KV, int ROLE>
__device__ __forceinline__ void data_body(Smem& sm, const int tid, const int lane, const int wave, const int c,
                                          const __amdgpu_buffer_rsrc_t rs,
                                          const unsigned short* __restrict__ kc, const unsigned short* __restrict__ vc,
                                          const unsigned short* __restrict__ wo, const unsigned short* __restrict__ w1,
                                          const unsigned short* __restrict__ b1, const unsigned short* __restrict__ w2,
                                          const unsigned short* __restrict__ x, const unsigned short* __restrict__ b2,
                                          unsigned short* __restrict__ out, char* ws) {
  constexpr bool HAS_MLP = (ROLE == 1);
  const int g = c >> 4, s = c & 15;
  RAWBAR();
  SCHED();
  u32x4 kv[8], vv[8];
  const size_t kvbase = ((size_t)g * SK + (SK - WIN) + s * 256 + wave * 32 + (lane >> 4)) * DH + (lane & 15) * 8;
  if constexpr (HAS_KV) {
#pragma unroll
    for (int j = 0; j < 8; ++j) kv[j] = ld_nt(kc + kvbase + (size_t)j * 4 * DH);
  }
  CFENCE();
  SCHED();
  RAWBAR();
  SCHED();
  if constexpr (HAS_KV) {
#pragma unroll
    for (int j = 0; j < 8; ++j) vv[j] = ld_nt(vc + kvbase + (size_t)j * 4 * DH);
  }
  SCHED();
  u32x4 w1v[4], w2v[4];
  unsigned short b1r[4];
  if constexpr (HAS_MLP) {
    const unsigned short* p1 = w1 + (size_t)(tid >> 2) * FF + c * 32 + (tid & 3) * 8;
    const unsigned short* p2 = w2 + (size_t)c * 32 * 576 + tid * 8;
#pragma unroll
    for (int i = 0; i < 4; ++i) w1v[i] = ld_nt(p1 + (size_t)i * 144 * FF);
#pragma unroll
    for (int i = 0; i < 4; ++i) w2v[i] = ld_nt(p2 + (size_t)i * 576 * 8);
#pragma unroll
    for (int i = 0; i < 4; ++i) b1r[i] = b1[c * 32 + tid / 72 + 8 * i];
  }
  CFENCE();
  SCHED();
  DSTAMP(10);
  BAR();
  SCHED();
  u32x4 wov[2];
  {
    const unsigned short* p = wo + (size_t)c * 16 * 576 + tid * 8;
    wov[0] = ld_nt(p);
    wov[1] = ld_nt(p + 576 * 8);
  }
  CFENCE();
  SCHED();
  const unsigned tag = sm.tag;
  if constexpr (HAS_KV) {
    unsigned qa[4], qb[4];
#pragma unroll
    for (int k = 0; k < 4; ++k) { qa[k] = sm.qbf[(lane & 15) * 4 + k]; qb[k] = sm.qbf[64 + (lane & 15) * 4 + k]; }
    float sa[8], sb[8];
#pragma unroll
    for (int j = 0; j < 8; ++j) {
      float a = dot2(kv[j].x, qa[0], 0.f), b = dot2(kv[j].x, qb[0], 0.f);
      a = dot2(kv[j].y, qa[1], a); b = dot2(kv[j].y, qb[1], b);
      a = dot2(kv[j].z, qa[2], a); b = dot2(kv[j].z, qb[2], b);
      a = dot2(kv[j].w, qa[3], a); b = dot2(kv[j].w, qb[3], b);
      sa[j] = sum_row16(a); sb[j] = sum_row16(b);
    }
    DSTAMP(12);
    float ma = -3.0e38f, mb = -3.0e38f;
#pragma unroll
    for (int j = 0; j < 8; ++j) {
      sa[j] = rbf(rbf(sa[j]) * 0.08838834764831845f);
      sb[j] = rbf(rbf(sb[j]) * 0.08838834764831845f);
      ma = fmaxf(ma, sa[j]); mb = fmaxf(mb, sb[j]);
    }
    ma = max_x32(max_x16(ma)); mb = max_x32(max_x16(mb));
    float la = 0.f, lb = 0.f;
    float oa[8], ob[8];
#pragma unroll
    for (int k = 0; k < 8; ++k) { oa[k] = 0.f; ob[k] = 0.f; }
#pragma unroll
    for (int j = 0; j < 8; ++j) {
      const float pa = __expf(sa[j] - ma), pb = __expf(sb[j] - mb);
      la += pa; lb += pb;
      fma8(oa, pa, vv[j]);
      fma8(ob, pb, vv[j]);
    }
    la = sum_x32(sum_x16(la)); lb = sum_x32(sum_x16(lb));
#pragma unroll
    for (int k = 0; k < 8; ++k) { oa[k] = sum_x32(sum_x16(oa[k])); ob[k] = sum_x32(sum_x16(ob[k])); }
    if (lane < 16) {
      *(float4*)&sm.att[wave][0][4 + lane * 8] = make_float4(oa[0], oa[1], oa[2], oa[3]);
      *(float4*)&sm.att[wave][0][8 + lane * 8] = make_float4(oa[4], oa[5], oa[6], oa[7]);
      *(float4*)&sm.att[wave][1][4 + lane * 8] = make_float4(ob[0], ob[1], ob[2], ob[3]);
      *(float4*)&sm.att[wave][1][8 + lane * 8] = make_float4(ob[4], ob[5], ob[6], ob[7]);
      if (lane == 0) { sm.att[wave][0][0] = ma; sm.att[wave][0][1] = la; sm.att[wave][1][0] = mb; sm.att[wave][1][1] = lb; }
    }
  }
  DSTAMP(13);
  BAR();
  if (tid < 128) {
    const int h = tid >> 6, d2 = (tid & 63) * 2;
    float M = sm.att[0][h][0];
#pragma unroll
    for (int w = 1; w < 8; ++w) M = fmaxf(M, sm.att[w][h][0]);
    float L = 0.f, o0 = 0.f, o1 = 0.f;
#pragma unroll
    for (int w = 0; w < 8; ++w) {
      const float e = __expf(sm.att[w][h][0] - M);
      L += e * sm.att[w][h][1];
      o0 += e * sm.att[w][h][4 + d2];
      o1 += e * sm.att[w][h][5 + d2];
    }
    const int blk = AG_OFF + ((2 * g + h) * 16 + s) * 144 * 8;
    u32x4 gv; gv.x = __float_as_uint(o0); gv.y = tag; gv.z = __float_as_uint(o1); gv.w = tag;
    st_sc1(rs, blk + (16 + d2) * 8, gv);
    if ((tid & 63) == 0) {
      u32x4 mv; mv.x = __float_as_uint(M); mv.y = tag; mv.z = __float_as_uint(L); mv.w = tag;
      st_sc1(rs, blk, mv);
    }
  }
  BAR();
  {
    const int r0 = tid / 72, cc = tid - r0 * 72;
    float acc[8];
#pragma unroll
    for (int k = 0; k < 8; ++k) acc[k] = 0.f;
    fma8(acc, sm.o16[r0], wov[0]);
    fma8(acc, sm.o16[r0 + 8], wov[1]);
    *(float4*)&sm.red[r0][cc * 8] = make_float4(acc[0], acc[1], acc[2], acc[3]);
    *(float4*)&sm.red[r0][cc * 8 + 4] = make_float4(acc[4], acc[5], acc[6], acc[7]);
  }
  BAR();
  if (tid < 288) {
    float v0 = 0.f, v1 = 0.f;
#pragma unroll
    for (int r = 0; r < 8; ++r) { const float2 t = *(const float2*)&sm.red[r][2 * tid]; v0 += t.x; v1 += t.y; }
    u32x4 gv; gv.x = __float_as_uint(v0); gv.y = tag; gv.z = __float_as_uint(v1); gv.w = tag;
    st_sc1(rs, WG_OFF + ((tid >> 2) * 256 + c) * 64 + (tid & 3) * 16, gv);
  }
  CFENCE();
  if constexpr (ROLE == 2) {
    const int r = c - NMLP;
    if (wave < 8) {
      int off[2];
      u32x4 a[2];
      off[0] = WG_OFF + (r * 256 + wave * 32 + (lane >> 1)) * 64 + (lane & 1) * 32;
      off[1] = off[0] + 16;
      gather<2>(rs, off, tag, a);
      float v[4] = {__uint_as_float(a[0].x), __uint_as_float(a[0].z), __uint_as_float(a[1].x), __uint_as_float(a[1].z)};
#pragma unroll
      for (int k = 0; k < 4; ++k) {
        float t = v[k];
        t += dppf<DPP_X2>(t); t += dppf<DPP_ROR4>(t); t += dppf<DPP_ROR8>(t);
        v[k] = sum_x32(sum_x16(t));
      }
      if (lane < 2) *(float4*)&sm.part[wave][lane * 4] = make_float4(v[0], v[1], v[2], v[3]);
    }
    BAR();
    if (tid < 4) {
      const unsigned xw = ((const unsigned*)x)[4 * r + tid];
      float v0 = 0.f, v1 = 0.f;
#pragma unroll
      for (int w = 0; w < 8; ++w) { v0 += sm.part[w][2 * tid]; v1 += sm.part[w][2 * tid + 1]; }
      u32x4 g0;
      g0.x = __float_as_uint(rbf(bf_lo(xw) + rbf(v0))); g0.y = tag;
      g0.z = __float_as_uint(rbf(bf_hi(xw) + rbf(v1))); g0.w = tag;
      st_sc1(rs, HG_OFF + (8 * r + 2 * tid) * 8, g0);
    }
    DSTAMP(6);
  }
  if constexpr (HAS_MLP) {
    if (wave < 5 && tid < 288) {
      int off[1];
      u32x4 a[1];
      off[0] = HG_OFF + tid * 16;
      gather<1>(rs, off, tag, a);
      *(float2*)&sm.h[2 * tid] = make_float2(__uint_as_float(a[0].x), __uint_as_float(a[0].z));
    }
    DSTAMP(7);
    CSTAMP(14);
    BAR();
    DSTAMP(12);
    {
      float hv[9];
      float sum = 0.f;
#pragma unroll
      for (int i = 0; i < 9; ++i) { hv[i] = sm.h[lane + 64 * i]; sum += hv[i]; }
      sum = sum_wave(sum);
      const float mean = sum * (1.f / 576.f);
      float sq = 0.f;
#pragma unroll
      for (int i = 0; i < 9; ++i) { const float d = hv[i] - mean; sq += d * d; }
      sq = sum_wave(sq);
      const float mb = rbf(mean);
      const float sd = rbf(sqrtf(rbf(rbf(sq * (1.f / 576.f)) + 1e-5f)));
      float acc[8];
#pragma unroll
      for (int k = 0; k < 8; ++k) acc[k] = 0.f;
#pragma unroll
      for (int i = 0; i < 4; ++i) fma8(acc, rbf(rbf(sm.h[(tid >> 2) + 144 * i] - mb) / sd), w1v[i]);
#pragma unroll
      for (int k = 0; k < 8; ++k) {
        float a = acc[k];
        a += dppf<DPP_ROR4>(a); a += dppf<DPP_ROR8>(a);
        acc[k] = sum_x32(sum_x16(a));
      }
      if (lane < 4) {
        *(float4*)&sm.w1red[wave][lane * 8] = make_float4(acc[0], acc[1], acc[2], acc[3]);
        *(float4*)&sm.w1red[wave][lane * 8 + 4] = make_float4(acc[4], acc[5], acc[6], acc[7]);
      }
    }
    BAR();
    DSTAMP(13);
    {
      const int r0 = tid / 72, cc = tid - r0 * 72;
      float acc[8];
#pragma unroll
      for (int k = 0; k < 8; ++k) acc[k] = 0.f;
#pragma unroll
      for (int i = 0; i < 4; ++i) {
        float v = 0.f;
#pragma unroll
        for (int w = 0; w < 9; ++w) v += sm.w1red[w][r0 + 8 * i];
        v = rbf(rbf(v) + bf_us(b1r[i]));
        v = fmaxf(v, 0.f);
        v = rbf(v * v);
        fma8(acc, v, w2v[i]);
      }
      *(float4*)&sm.red[r0][cc * 8] = make_float4(acc[0], acc[1], acc[2], acc[3]);
      *(float4*)&sm.red[r0][cc * 8 + 4] = make_float4(acc[4], acc[5], acc[6], acc[7]);
    }
    BAR();
    DSTAMP(8);
    CSTAMP(15);
    if (tid < 288) {
      float v0 = 0.f, v1 = 0.f;
#pragma unroll
      for (int r = 0; r < 8; ++r) { const float2 t = *(const float2*)&sm.red[r][2 * tid]; v0 += t.x; v1 += t.y; }
      u32x4 gv; gv.x = __float_as_uint(v0); gv.y = tag; gv.z = __float_as_uint(v1); gv.w = tag;
      st_sc1(rs, MG_OFF + ((tid >> 1) * 144 + c) * 32 + (tid & 1) * 16, gv);
    }
    if (wave < 5) {
      float v0 = 0.f, v1 = 0.f;
      if (tid < 288) {
        int off[1];
        u32x4 a[1];
        off[0] = MG_OFF + c * 144 * 32 + tid * 16;
        gather<1>(rs, off, tag, a);
        v0 = __uint_as_float(a[0].x); v1 = __uint_as_float(a[0].z);
      }
      v0 += dppf<DPP_X2>(v0); v0 += dppf<DPP_ROR4>(v0); v0 += dppf<DPP_ROR8>(v0); v0 = sum_x32(sum_x16(v0));
      v1 += dppf<DPP_X2>(v1); v1 += dppf<DPP_ROR4>(v1); v1 += dppf<DPP_ROR8>(v1); v1 = sum_x32(sum_x16(v1));
      if (lane < 2) *(float2*)&sm.part[wave][lane * 2] = make_float2(v0, v1);
    }
    BAR();
    if (tid < 2) {
      const unsigned bw = ((const unsigned*)b2)[2 * c + tid];
      float v0 = 0.f, v1 = 0.f;
#pragma unroll
      for (int w = 0; w < 5; ++w) { v0 += sm.part[w][2 * tid]; v1 += sm.part[w][2 * tid + 1]; }
      const float h0 = sm.h[4 * c + 2 * tid], h1 = sm.h[4 * c + 2 * tid + 1];
      ((unsigned*)out)[2 * c + tid] = (unsigned)to_bf_us(h0 + rbf(rbf(v0) + bf_lo(bw))) |
                                      ((unsigned)to_bf_us(h1 + rbf(rbf(v1) + bf_hi(bw))) << 16);
    }
    if (c == 0 && tid == 0) __builtin_amdgcn_raw_buffer_store_b32(tag, rs, EPOCH_OFF, 0, AUX_SC1);
    DSTAMP(9);
  }
}

}

__global__ void __launch_bounds__(640, 3)
fused_decode_block(const unsigned short* __restrict__ x, const unsigned short* __restrict__ kc,
                   const unsigned short* __restrict__ vc, const unsigned short* __restrict__ wq,
                   const unsigned short* __restrict__ wo, const unsigned short* __restrict__ w1,
                   const unsigned short* __restrict__ b1, const unsigned short* __restrict__ w2,
                   const unsigned short* __restrict__ b2, unsigned short* __restrict__ out, char* ws) {
  __shared__ __attribute__((aligned(16))) Smem sm;
  const int tid = threadIdx.x;
  const int lane = tid & 63;
  const int wave = __builtin_amdgcn_readfirstlane(tid >> 6);
  const int c = blockIdx.x;
  const int g = c >> 4;
  const __amdgpu_buffer_rsrc_t rs = __builtin_amdgcn_make_buffer_rsrc((void*)ws, 0, WS_BYTES, 0x00020000);

  if (wave < 8) {
    if (c < NMLP) data_body<true, 1>(sm, tid, lane, wave, c, rs, kc, vc, wo, w1, b1, w2, x, b2, out, ws);
    else if (c < NMLP + 72) data_body<true, 2>(sm, tid, lane, wave, c, rs, kc, vc, wo, w1, b1, w2, x, b2, out, ws);
    else data_body<true, 0>(sm, tid, lane, wave, c, rs, kc, vc, wo, w1, b1, w2, x, b2, out, ws);
  } else if (wave == 8) {
    if (c < NMLP) data_body<false, 1>(sm, tid, lane, wave, c, rs, kc, vc, wo, w1, b1, w2, x, b2, out, ws);
    else if (c < NMLP + 72) data_body<false, 2>(sm, tid, lane, wave, c, rs, kc, vc, wo, w1, b1, w2, x, b2, out, ws);
    else data_body<false, 0>(sm, tid, lane, wave, c, rs, kc, vc, wo, w1, b1, w2, x, b2, out, ws);
  } else {
    STAMP(0);
    const int s = c & 15, cs = s & 3, kq = s >> 2;
    const unsigned epoch = __builtin_amdgcn_raw_buffer_load_b32(rs, EPOCH_OFF, 0, AUX_SC1);
    unsigned xd[5];
#pragma unroll
    for (int i = 0; i < 5; ++i) {
      const int idx = lane + 64 * i;
      xd[i] = ((const unsigned*)x)[idx < 288 ? idx : 287];
    }
    u32x4 wqv[18];
    {
      const unsigned short* p = wq + (size_t)(kq * 144 + (lane >> 3)) * 4096 + g * 256 + cs * 64 + (lane & 7) * 8;
#pragma unroll
      for (int i = 0; i < 18; ++i) wqv[i] = ld_nt(p + (size_t)i * 8 * 4096);
    }
    CFENCE();
    SCHED();
    RAWBAR();
    SCHED();
    const unsigned tag = epoch + 1u;
    {
      float sum = 0.f;
#pragma unroll
      for (int i = 0; i < 5; ++i) sum += (lane + 64 * i < 288) ? (bf_lo(xd[i]) + bf_hi(xd[i])) : 0.f;
      sum = sum_wave(sum);
      const float mean = sum * (1.f / 576.f);
      float sq = 0.f;
#pragma unroll
      for (int i = 0; i < 5; ++i) {
        const float a = bf_lo(xd[i]) - mean, b = bf_hi(xd[i]) - mean;
        sq += (lane + 64 * i < 288) ? (a * a + b * b) : 0.f;
      }
      sq = sum_wave(sq);
      const float mb = rbf(mean);
      const float sd = rbf(sqrtf(rbf(rbf(sq * (1.f / 576.f)) + 1e-5f)));
#pragma unroll
      for (int i = 0; i < 5; ++i) {
        if (lane + 64 * i < 288) {
          *(float2*)&sm.xn[2 * (lane + 64 * i)] =
              make_float2(rbf(rbf(bf_lo(xd[i]) - mb) / sd), rbf(rbf(bf_hi(xd[i]) - mb) / sd));
        }
      }
    }
    asm volatile("s_waitcnt lgkmcnt(0)" ::: "memory");
    STAMP(11);
    {
      float acc[8];
#pragma unroll
      for (int k = 0; k < 8; ++k) acc[k] = 0.f;
#pragma unroll
      for (int i = 0; i < 18; ++i) fma8(acc, sm.xn[kq * 144 + i * 8 + (lane >> 3)], wqv[i]);
#pragma unroll
      for (int k = 0; k < 8; ++k) {
        float a = acc[k];
        a += dppf<DPP_ROR8>(a);
        acc[k] = sum_x32(sum_x16(a));
      }
      if (lane < 8) {
        const int off = QG_OFF + (kq * 4096 + g * 256 + cs * 64 + lane * 8) * 8;
#pragma unroll
        for (int k = 0; k < 4; ++k) {
          u32x4 gv; gv.x = __float_as_uint(acc[2 * k]); gv.y = tag; gv.z = __float_as_uint(acc[2 * k + 1]); gv.w = tag;
          st_sc1(rs, off + k * 16, gv);
        }
      }
    }
    STAMP(1);
    RAWBAR();
    {
      float q4[4];
      {
        int off[8];
        u32x4 r[8];
#pragma unroll
        for (int k = 0; k < 4; ++k) {
          off[2 * k] = QG_OFF + (k * 4096 + g * 256 + lane * 4) * 8;
          off[2 * k + 1] = off[2 * k] + 16;
        }
        gather<8>(rs, off, tag, r);
#pragma unroll
        for (int k = 0; k < 4; ++k) q4[k] = 0.f;
#pragma unroll
        for (int k = 0; k < 4; ++k) {
          q4[0] += __uint_as_float(r[2 * k].x); q4[1] += __uint_as_float(r[2 * k].z);
          q4[2] += __uint_as_float(r[2 * k + 1].x); q4[3] += __uint_as_float(r[2 * k + 1].z);
        }
      }
      sm.qbf[lane * 2] = (unsigned)to_bf_us(q4[0]) | ((unsigned)to_bf_us(q4[1]) << 16);
      sm.qbf[lane * 2 + 1] = (unsigned)to_bf_us(q4[2]) | ((unsigned)to_bf_us(q4[3]) << 16);
      if (lane == 0) sm.tag = tag;
    }
    STAMP(2);
    BAR();
    BAR();
    STAMP(3);
    {
      const int hblk = AG_OFF + (c >> 3) * 16 * 144 * 8;
      const int j16 = (c & 7) * 16;
      {
        int off[3];
        u32x4 r[3];
        off[0] = hblk + (lane & 15) * 144 * 8;
        off[1] = hblk + ((lane >> 3) * 144 + 16 + j16 + (lane & 7) * 2) * 8;
        off[2] = hblk + ((8 + (lane >> 3)) * 144 + 16 + j16 + (lane & 7) * 2) * 8;
        gather<3>(rs, off, tag, r);
        if (lane < 16) { sm.raw[256 + lane * 2] = __uint_as_float(r[0].x); sm.raw[257 + lane * 2] = __uint_as_float(r[0].z); }
        sm.raw[(lane >> 3) * 16 + (lane & 7) * 2] = __uint_as_float(r[1].x);
        sm.raw[(lane >> 3) * 16 + (lane & 7) * 2 + 1] = __uint_as_float(r[1].z);
        sm.raw[(8 + (lane >> 3)) * 16 + (lane & 7) * 2] = __uint_as_float(r[2].x);
        sm.raw[(8 + (lane >> 3)) * 16 + (lane & 7) * 2 + 1] = __uint_as_float(r[2].z);
      }
      asm volatile("s_waitcnt lgkmcnt(0)" ::: "memory");
      if (lane < 16) {
        float M = sm.raw[256];
#pragma unroll
        for (int sp = 1; sp < 16; ++sp) M = fmaxf(M, sm.raw[256 + sp * 2]);
        float L = 0.f, o = 0.f;
#pragma unroll
        for (int sp = 0; sp < 16; ++sp) {
          const float e = __expf(sm.raw[256 + sp * 2] - M);
          L += e * sm.raw[257 + sp * 2];
          o += e * sm.raw[sp * 16 + lane];
        }
        sm.o16[lane] = rbf(o / L);
      }
    }
    STAMP(4);
    BAR();
    BAR();
    STAMP(5);
    if (c < NMLP) {
      BAR();
      BAR();
      BAR();
      BAR();
    } else if (c < NMLP + 72) {
      BAR();
    }
  }
}

extern "C" void kernel_launch(void* const* d_in, const int* in_sizes, int n_in,
                              void* d_out, int out_size, void* d_ws, size_t ws_size,
                              hipStream_t stream) {
  (void)in_sizes; (void)n_in; (void)out_size; (void)ws_size;
  fused_decode_block<<<256, 640, 0, stream>>>(
      (const unsigned short*)d_in[0], (const unsigned short*)d_in[1], (const unsigned short*)d_in[2],
      (const unsigned short*)d_in[3], (const unsigned short*)d_in[4], (const unsigned short*)d_in[6],
      (const unsigned short*)d_in[7], (const unsigned short*)d_in[8], (const unsigned short*)d_in[9],
      (unsigned short*)d_out, (char*)d_ws);
}
```

```cpp
#include <hip/hip_runtime.h>
#include <hip/hip_bf16.h>
#include <stdint.h>

typedef __attribute__((ext_vector_type(4))) unsigned int u32x4;
typedef __attribute__((ext_vector_type(2))) unsigned int u32x2;
typedef __attribute__((ext_vector_type(2))) __bf16 bf16x2_t;

namespace {

constexpr int SK = 65536;
constexpr int WIN = 4096;
constexpr int DH = 128;
constexpr int FF = 4608;
constexpr int NMLP = 144;

constexpr int EPOCH_OFF = 0;
constexpr int QG_OFF = 4096;
constexpr int AG_OFF = QG_OFF + 4 * 4096 * 8;
constexpr int WG_OFF = AG_OFF + 32 * 16 * 144 * 8;
constexpr int HG_OFF = WG_OFF + 72 * 256 * 64;
constexpr int MG_OFF = HG_OFF + 8192;
constexpr int WS_BYTES = MG_OFF + 144 * 144 * 32;

constexpr int AUX_SC1 = 16;
constexpr unsigned SPIN_CAP = 200000u;

__device__ __forceinline__ float bf_lo(unsigned u) { return __uint_as_float(u << 16); }
__device__ __forceinline__ float bf_hi(unsigned u) { return __uint_as_float(u & 0xffff0000u); }
__device__ __forceinline__ float bf_us(unsigned short u) { return __uint_as_float(((unsigned)u) << 16); }
__device__ __forceinline__ float rbf(float f) { return (float)(__bf16)f; }
__device__ __forceinline__ unsigned short to_bf_us(float f) { return __builtin_bit_cast(unsigned short, (__bf16)f); }
__device__ __forceinline__ float dot2(unsigned k, unsigned q, float acc) {
  return __builtin_amdgcn_fdot2_f32_bf16(__builtin_bit_cast(bf16x2_t, k), __builtin_bit_cast(bf16x2_t, q), acc, false);
}
#define BAR() asm volatile("s_waitcnt lgkmcnt(0)\n\ts_barrier" ::: "memory")
#define RAWBAR() asm volatile("s_barrier" ::: "memory")
#define CFENCE() asm volatile("" ::: "memory")
#define SCHED() __builtin_amdgcn_sched_barrier(0)
#define STAMP(i) do { } while (0)
#define DSTAMP(i) do { } while (0)
#define CSTAMP(i) do { } while (0)

template <int CTRL>
__device__ __forceinline__ float dppf(float x) {
  return __int_as_float(__builtin_amdgcn_update_dpp(0, __float_as_int(x), CTRL, 0xf, 0xf, true));
}
constexpr int DPP_X1 = 0xB1, DPP_X2 = 0x4E, DPP_ROR4 = 0x124, DPP_ROR8 = 0x128;
__device__ __forceinline__ float sum_x16(float x) {
  auto r = __builtin_amdgcn_permlane16_swap(__float_as_uint(x), __float_as_uint(x), false, false);
  return __uint_as_float(r[0]) + __uint_as_float(r[1]);
}
__device__ __forceinline__ float sum_x32(float x) {
  auto r = __builtin_amdgcn_permlane32_swap(__float_as_uint(x), __float_as_uint(x), false, false);
  return __uint_as_float(r[0]) + __uint_as_float(r[1]);
}
__device__ __forceinline__ float max_x16(float x) {
  auto r = __builtin_amdgcn_permlane16_swap(__float_as_uint(x), __float_as_uint(x), false, false);
  return fmaxf(__uint_as_float(r[0]), __uint_as_float(r[1]));
}
__device__ __forceinline__ float max_x32(float x) {
  auto r = __builtin_amdgcn_permlane32_swap(__float_as_uint(x), __float_as_uint(x), false, false);
  return fmaxf(__uint_as_float(r[0]), __uint_as_float(r[1]));
}
__device__ __forceinline__ float sum_row16(float x) {
  x += dppf<DPP_X1>(x); x += dppf<DPP_X2>(x); x += dppf<DPP_ROR4>(x); x += dppf<DPP_ROR8>(x);
  return x;
}
__device__ __forceinline__ float sum_wave(float x) { return sum_x32(sum_x16(sum_row16(x))); }

__device__ __forceinline__ u32x4 ld_nt(const unsigned short* p) { return __builtin_nontemporal_load((const u32x4*)p); }
__device__ __forceinline__ u32x4 ld_sc1(__amdgpu_buffer_rsrc_t rs, int off) {
  return __builtin_amdgcn_raw_buffer_load_b128(rs, off, 0, AUX_SC1);
}
__device__ __forceinline__ void st_sc1(__amdgpu_buffer_rsrc_t rs, int off, u32x4 v) {
  __builtin_amdgcn_raw_buffer_store_b128(v, rs, off, 0, AUX_SC1);
}
__device__ __forceinline__ void fma8(float (&acc)[8], float a, u32x4 w) {
  acc[0] += a * bf_lo(w.x); acc[1] += a * bf_hi(w.x);
  acc[2] += a * bf_lo(w.y); acc[3] += a * bf_hi(w.y);
  acc[4] += a * bf_lo(w.z); acc[5] += a * bf_hi(w.z);
  acc[6] += a * bf_lo(w.w); acc[7] += a * bf_hi(w.w);
}

template <int N>
__device__ __forceinline__ void gather(const __amdgpu_buffer_rsrc_t rs, const int (&off)[N], const unsigned tag,
                                       u32x4 (&res)[N]) {
  u32x4 a[N], b[N];
#pragma unroll
  for (int k = 0; k < N; ++k) a[k] = ld_sc1(rs, off[k]);
  for (unsigned spin = 0; spin < SPIN_CAP; ++spin) {
    CFENCE();
#pragma unroll
    for (int k = 0; k < N; ++k) b[k] = ld_sc1(rs, off[k]);
    bool ok = true;
#pragma unroll
    for (int k = 0; k < N; ++k) ok &= (a[k].y == tag) & (a[k].w == tag);
    if (__all(ok)) {
#pragma unroll
      for (int k = 0; k < N; ++k) res[k] = a[k];
      return;
    }
    CFENCE();
#pragma unroll
    for (int k = 0; k < N; ++k) a[k] = ld_sc1(rs, off[k]);
    ok = true;
#pragma unroll
    for (int k = 0; k < N; ++k) ok &= (b[k].y == tag) & (b[k].w == tag);
    if (__all(ok)) {
#pragma unroll
      for (int k = 0; k < N; ++k) res[k] = b[k];
      return;
    }
  }
#pragma unroll
  for (int k = 0; k < N; ++k) res[k] = a[k];
}

struct Smem {
  float red[8][576];
  float att[8][2][132];
  float xn[576];
  unsigned qbf[128];
  float raw[16 * 16 + 32];
  float o16[16];
  float w1red[9][32];
  float h[576];
  float part[8][8];
  unsigned tag;
};

__device__ __forceinline__ void data_body(Smem& sm, const int tid, const int lane, const int wave, const int c,
                                          const __amdgpu_buffer_rsrc_t rs,
                                          const unsigned short* __restrict__ kc, const unsigned short* __restrict__ vc,
                                          const unsigned short* __restrict__ wo, const unsigned short* __restrict__ w1,
                                          const unsigned short* __restrict__ b1, const unsigned short* __restrict__ w2,
                                          const unsigned short* __restrict__ x, const unsigned short* __restrict__ b2,
                                          unsigned short* __restrict__ out, char* ws) {
  const bool HAS_MLP = (c < NMLP);
  const bool IS_RED = (c >= NMLP) && (c < NMLP + 72);
  const int kvw = wave < 8 ? wave : 7;
  const int g = c >> 4, s = c & 15;
  RAWBAR();
  SCHED();
  u32x4 kv[8], vv[8];
  const size_t kvbase = ((size_t)g * SK + (SK - WIN) + s * 256 + kvw * 32 + (lane >> 4)) * DH + (lane & 15) * 8;
#pragma unroll
  for (int j = 0; j < 8; ++j) kv[j] = ld_nt(kc + kvbase + (size_t)j * 4 * DH);
  CFENCE();
  SCHED();
  RAWBAR();
  SCHED();
#pragma unroll
  for (int j = 0; j < 8; ++j) vv[j] = ld_nt(vc + kvbase + (size_t)j * 4 * DH);
  SCHED();
  u32x4 w1v[4], w2v[4];
  unsigned short b1r[4];
  if (HAS_MLP) {
    const unsigned short* p1 = w1 + (size_t)(tid >> 2) * FF + c * 32 + (tid & 3) * 8;
    const unsigned short* p2 = w2 + (size_t)c * 32 * 576 + tid * 8;
#pragma unroll
    for (int i = 0; i < 4; ++i) w1v[i] = ld_nt(p1 + (size_t)i * 144 * FF);
#pragma unroll
    for (int i = 0; i < 4; ++i) w2v[i] = ld_nt(p2 + (size_t)i * 576 * 8);
#pragma unroll
    for (int i = 0; i < 4; ++i) b1r[i] = b1[c * 32 + tid / 72 + 8 * i];
  }
  CFENCE();
  SCHED();
  DSTAMP(10);
  BAR();
  SCHED();
  u32x4 wov[2];
  {
    const unsigned short* p = wo + (size_t)c * 16 * 576 + tid * 8;
    wov[0] = ld_nt(p);
    wov[1] = ld_nt(p + 576 * 8);
  }
  CFENCE();
  SCHED();
  const unsigned tag = sm.tag;
  {
    unsigned qa[4], qb[4];
#pragma unroll
    for (int k = 0; k < 4; ++k) { qa[k] = sm.qbf[(lane & 15) * 4 + k]; qb[k] = sm.qbf[64 + (lane & 15) * 4 + k]; }
    float sa[8], sb[8];
#pragma unroll
    for (int j = 0; j < 8; ++j) {
      float a = dot2(kv[j].x, qa[0], 0.f), b = dot2(kv[j].x, qb[0], 0.f);
      a = dot2(kv[j].y, qa[1], a); b = dot2(kv[j].y, qb[1], b);
      a = dot2(kv[j].z, qa[2], a); b = dot2(kv[j].z, qb[2], b);
      a = dot2(kv[j].w, qa[3], a); b = dot2(kv[j].w, qb[3], b);
      sa[j] = sum_row16(a); sb[j] = sum_row16(b);
    }
    DSTAMP(12);
    float ma = -3.0e38f, mb = -3.0e38f;
#pragma unroll
    for (int j = 0; j < 8; ++j) {
      sa[j] = rbf(rbf(sa[j]) * 0.08838834764831845f);
      sb[j] = rbf(rbf(sb[j]) * 0.08838834764831845f);
      ma = fmaxf(ma, sa[j]); mb = fmaxf(mb, sb[j]);
    }
    ma = max_x32(max_x16(ma)); mb = max_x32(max_x16(mb));
    float la = 0.f, lb = 0.f;
    float oa[8], ob[8];
#pragma unroll
    for (int k = 0; k < 8; ++k) { oa[k] = 0.f; ob[k] = 0.f; }
#pragma unroll
    for (int j = 0; j < 8; ++j) {
      const float pa = __expf(sa[j] - ma), pb = __expf(sb[j] - mb);
      la += pa; lb += pb;
      fma8(oa, pa, vv[j]);
      fma8(ob, pb, vv[j]);
    }
    la = sum_x32(sum_x16(la)); lb = sum_x32(sum_x16(lb));
#pragma unroll
    for (int k = 0; k < 8; ++k) { oa[k] = sum_x32(sum_x16(oa[k])); ob[k] = sum_x32(sum_x16(ob[k])); }
    if (lane < 16 && wave < 8) {
      *(float4*)&sm.att[wave][0][4 + lane * 8] = make_float4(oa[0], oa[1], oa[2], oa[3]);
      *(float4*)&sm.att[wave][0][8 + lane * 8] = make_float4(oa[4], oa[5], oa[6], oa[7]);
      *(float4*)&sm.att[wave][1][4 + lane * 8] = make_float4(ob[0], ob[1], ob[2], ob[3]);
      *(float4*)&sm.att[wave][1][8 + lane * 8] = make_float4(ob[4], ob[5], ob[6], ob[7]);
      if (lane == 0) { sm.att[wave][0][0] = ma; sm.att[wave][0][1] = la; sm.att[wave][1][0] = mb; sm.att[wave][1][1] = lb; }
    }
  }
  DSTAMP(13);
  BAR();
  if (tid < 128) {
    const int h = tid >> 6, d2 = (tid & 63) * 2;
    float M = sm.att[0][h][0];
#pragma unroll
    for (int w = 1; w < 8; ++w) M = fmaxf(M, sm.att[w][h][0]);
    float L = 0.f, o0 = 0.f, o1 = 0.f;
#pragma unroll
    for (int w = 0; w < 8; ++w) {
      const float e = __expf(sm.att[w][h][0] - M);
      L += e * sm.att[w][h][1];
      o0 += e * sm.att[w][h][4 + d2];
      o1 += e * sm.att[w][h][5 + d2];
    }
    const int blk = AG_OFF + ((2 * g + h) * 16 + s) * 144 * 8;
    u32x4 gv; gv.x = __float_as_uint(o0); gv.y = tag; gv.z = __float_as_uint(o1); gv.w = tag;
    st_sc1(rs, blk + (16 + d2) * 8, gv);
    if ((tid & 63) == 0) {
      u32x4 mv; mv.x = __float_as_uint(M); mv.y = tag; mv.z = __float_as_uint(L); mv.w = tag;
      st_sc1(rs, blk, mv);
    }
  }
  BAR();
  {
    const int r0 = tid / 72, cc = tid - r0 * 72;
    float acc[8];
#pragma unroll
    for (int k = 0; k < 8; ++k) acc[k] = 0.f;
    fma8(acc, sm.o16[r0], wov[0]);
    fma8(acc, sm.o16[r0 + 8], wov[1]);
    *(float4*)&sm.red[r0][cc * 8] = make_float4(acc[0], acc[1], acc[2], acc[3]);
    *(float4*)&sm.red[r0][cc * 8 + 4] = make_float4(acc[4], acc[5], acc[6], acc[7]);
  }
  BAR();
  if (tid < 288) {
    float v0 = 0.f, v1 = 0.f;
#pragma unroll
    for (int r = 0; r < 8; ++r) { const float2 t = *(const float2*)&sm.red[r][2 * tid]; v0 += t.x; v1 += t.y; }
    u32x4 gv; gv.x = __float_as_uint(v0); gv.y = tag; gv.z = __float_as_uint(v1); gv.w = tag;
    st_sc1(rs, WG_OFF + ((tid >> 2) * 256 + c) * 64 + (tid & 3) * 16, gv);
  }
  CFENCE();
  if (IS_RED) {
    const int r = c - NMLP;
    if (wave < 8) {
      int off[2];
      u32x4 a[2];
      off[0] = WG_OFF + (r * 256 + wave * 32 + (lane >> 1)) * 64 + (lane & 1) * 32;
      off[1] = off[0] + 16;
      gather<2>(rs, off, tag, a);
      float v[4] = {__uint_as_float(a[0].x), __uint_as_float(a[0].z), __uint_as_float(a[1].x), __uint_as_float(a[1].z)};
#pragma unroll
      for (int k = 0; k < 4; ++k) {
        float t = v[k];
        t += dppf<DPP_X2>(t); t += dppf<DPP_ROR4>(t); t += dppf<DPP_ROR8>(t);
        v[k] = sum_x32(sum_x16(t));
      }
      if (lane < 2) *(float4*)&sm.part[wave][lane * 4] = make_float4(v[0], v[1], v[2], v[3]);
    }
    BAR();
    if (tid < 4) {
      const unsigned xw = ((const unsigned*)x)[4 * r + tid];
      float v0 = 0.f, v1 = 0.f;
#pragma unroll
      for (int w = 0; w < 8; ++w) { v0 += sm.part[w][2 * tid]; v1 += sm.part[w][2 * tid + 1]; }
      u32x4 g0;
      g0.x = __float_as_uint(rbf(bf_lo(xw) + rbf(v0))); g0.y = tag;
      g0.z = __float_as_uint(rbf(bf_hi(xw) + rbf(v1))); g0.w = tag;
      st_sc1(rs, HG_OFF + (8 * r + 2 * tid) * 8, g0);
    }
    DSTAMP(6);
  }
  if (HAS_MLP) {
    if (wave < 5 && tid < 288) {
      int off[1];
      u32x4 a[1];
      off[0] = HG_OFF + tid * 16;
      gather<1>(rs, off, tag, a);
      *(float2*)&sm.h[2 * tid] = make_float2(__uint_as_float(a[0].x), __uint_as_float(a[0].z));
    }
    DSTAMP(7);
    CSTAMP(14);
    BAR();
    DSTAMP(12);
    {
      float hv[9];
      float sum = 0.f;
#pragma unroll
      for (int i = 0; i < 9; ++i) { hv[i] = sm.h[lane + 64 * i]; sum += hv[i]; }
      sum = sum_wave(sum);
      const float mean = sum * (1.f / 576.f);
      float sq = 0.f;
#pragma unroll
      for (int i = 0; i < 9; ++i) { const float d = hv[i] - mean; sq += d * d; }
      sq = sum_wave(sq);
      const float mb = rbf(mean);
      const float sd = rbf(sqrtf(rbf(rbf(sq * (1.f / 576.f)) + 1e-5f)));
      float acc[8];
#pragma unroll
      for (int k = 0; k < 8; ++k) acc[k] = 0.f;
#pragma unroll
      for (int i = 0; i < 4; ++i) fma8(acc, rbf(rbf(sm.h[(tid >> 2) + 144 * i] - mb) / sd), w1v[i]);
#pragma unroll
      for (int k = 0; k < 8; ++k) {
        float a = acc[k];
        a += dppf<DPP_ROR4>(a); a += dppf<DPP_ROR8>(a);
        acc[k] = sum_x32(sum_x16(a));
      }
      if (lane < 4) {
        *(float4*)&sm.w1red[wave][lane * 8] = make_float4(acc[0], acc[1], acc[2], acc[3]);
        *(float4*)&sm.w1red[wave][lane * 8 + 4] = make_float4(acc[4], acc[5], acc[6], acc[7]);
      }
    }
    BAR();
    DSTAMP(13);
    {
      const int r0 = tid / 72, cc = tid - r0 * 72;
      float acc[8];
#pragma unroll
      for (int k = 0; k < 8; ++k) acc[k] = 0.f;
#pragma unroll
      for (int i = 0; i < 4; ++i) {
        float v = 0.f;
#pragma unroll
        for (int w = 0; w < 9; ++w) v += sm.w1red[w][r0 + 8 * i];
        v = rbf(rbf(v) + bf_us(b1r[i]));
        v = fmaxf(v, 0.f);
        v = rbf(v * v);
        fma8(acc, v, w2v[i]);
      }
      *(float4*)&sm.red[r0][cc * 8] = make_float4(acc[0], acc[1], acc[2], acc[3]);
      *(float4*)&sm.red[r0][cc * 8 + 4] = make_float4(acc[4], acc[5], acc[6], acc[7]);
    }
    BAR();
    DSTAMP(8);
    CSTAMP(15);
    if (tid < 288) {
      float v0 = 0.f, v1 = 0.f;
#pragma unroll
      for (int r = 0; r < 8; ++r) { const float2 t = *(const float2*)&sm.red[r][2 * tid]; v0 += t.x; v1 += t.y; }
      u32x4 gv; gv.x = __float_as_uint(v0); gv.y = tag; gv.z = __float_as_uint(v1); gv.w = tag;
      st_sc1(rs, MG_OFF + ((tid >> 1) * 144 + c) * 32 + (tid & 1) * 16, gv);
    }
    if (wave < 5) {
      float v0 = 0.f, v1 = 0.f;
      if (tid < 288) {
        int off[1];
        u32x4 a[1];
        off[0] = MG_OFF + c * 144 * 32 + tid * 16;
        gather<1>(rs, off, tag, a);
        v0 = __uint_as_float(a[0].x); v1 = __uint_as_float(a[0].z);
      }
      v0 += dppf<DPP_X2>(v0); v0 += dppf<DPP_ROR4>(v0); v0 += dppf<DPP_ROR8>(v0); v0 = sum_x32(sum_x16(v0));
      v1 += dppf<DPP_X2>(v1); v1 += dppf<DPP_ROR4>(v1); v1 += dppf<DPP_ROR8>(v1); v1 = sum_x32(sum_x16(v1));
      if (lane < 2) *(float2*)&sm.part[wave][lane * 2] = make_float2(v0, v1);
    }
    BAR();
    if (tid < 2) {
      const unsigned bw = ((const unsigned*)b2)[2 * c + tid];
      float v0 = 0.f, v1 = 0.f;
#pragma unroll
      for (int w = 0; w < 5; ++w) { v0 += sm.part[w][2 * tid]; v1 += sm.part[w][2 * tid + 1]; }
      const float h0 = sm.h[4 * c + 2 * tid], h1 = sm.h[4 * c + 2 * tid + 1];
      ((unsigned*)out)[2 * c + tid] = (unsigned)to_bf_us(h0 + rbf(rbf(v0) + bf_lo(bw))) |
                                      ((unsigned)to_bf_us(h1 + rbf(rbf(v1) + bf_hi(bw))) << 16);
    }
    if (c == 0 && tid == 0) __builtin_amdgcn_raw_buffer_store_b32(tag, rs, EPOCH_OFF, 0, AUX_SC1);
    DSTAMP(9);
  }
}

}

__global__ void __launch_bounds__(640, 3)
fused_decode_block(const unsigned short* __restrict__ x, const unsigned short* __restrict__ kc,
                   const unsigned short* __restrict__ vc, const unsigned short* __restrict__ wq,
                   const unsigned short* __restrict__ wo, const unsigned short* __restrict__ w1,
                   const unsigned short* __restrict__ b1, const unsigned short* __restrict__ w2,
                   const unsigned short* __restrict__ b2, unsigned short* __restrict__ out, char* ws) {
  __shared__ __attribute__((aligned(16))) Smem sm;
  const int tid = threadIdx.x;
  const int lane = tid & 63;
  const int wave = __builtin_amdgcn_readfirstlane(tid >> 6);
  const int c = blockIdx.x;
  const int g = c >> 4;
  const __amdgpu_buffer_rsrc_t rs = __builtin_amdgcn_make_buffer_rsrc((void*)ws, 0, WS_BYTES, 0x00020000);

  if (wave < 9) {
    data_body(sm, tid, lane, wave, c, rs, kc, vc, wo, w1, b1, w2, x, b2, out, ws);
  } else {
    STAMP(0);
    const int s = c & 15, cs = s & 3, kq = s >> 2;
    const unsigned epoch = __builtin_amdgcn_raw_buffer_load_b32(rs, EPOCH_OFF, 0, AUX_SC1);
    unsigned xd[5];
#pragma unroll
    for (int i = 0; i < 5; ++i) {
      const int idx = lane + 64 * i;
      xd[i] = ((const unsigned*)x)[idx < 288 ? idx : 287];
    }
    u32x4 wqv[18];
    {
      const unsigned short* p = wq + (size_t)(kq * 144 + (lane >> 3)) * 4096 + g * 256 + cs * 64 + (lane & 7) * 8;
#pragma unroll
      for (int i = 0; i < 18; ++i) wqv[i] = ld_nt(p + (size_t)i * 8 * 4096);
    }
    CFENCE();
    SCHED();
    RAWBAR();
    SCHED();
    const unsigned tag = epoch + 1u;
    {
      float sum = 0.f;
#pragma unroll
      for (int i = 0; i < 5; ++i) sum += (lane + 64 * i < 288) ? (bf_lo(xd[i]) + bf_hi(xd[i])) : 0.f;
      sum = sum_wave(sum);
      const float mean = sum * (1.f / 576.f);
      float sq = 0.f;
#pragma unroll
      for (int i = 0; i < 5; ++i) {
        const float a = bf_lo(xd[i]) - mean, b = bf_hi(xd[i]) - mean;
        sq += (lane + 64 * i < 288) ? (a * a + b * b) : 0.f;
      }
      sq = sum_wave(sq);
      const float mb = rbf(mean);
      const float sd = rbf(sqrtf(rbf(rbf(sq * (1.f / 576.f)) + 1e-5f)));
#pragma unroll
      for (int i = 0; i < 5; ++i) {
        if (lane + 64 * i < 288) {
          *(float2*)&sm.xn[2 * (lane + 64 * i)] =
              make_float2(rbf(rbf(bf_lo(xd[i]) - mb) / sd), rbf(rbf(bf_hi(xd[i]) - mb) / sd));
        }
      }
    }
    asm volatile("s_waitcnt lgkmcnt(0)" ::: "memory");
    STAMP(11);
    {
      float acc[8];
#pragma unroll
      for (int k = 0; k < 8; ++k) acc[k] = 0.f;
#pragma unroll
      for (int i = 0; i < 18; ++i) fma8(acc, sm.xn[kq * 144 + i * 8 + (lane >> 3)], wqv[i]);
#pragma unroll
      for (int k = 0; k < 8; ++k) {
        float a = acc[k];
        a += dppf<DPP_ROR8>(a);
        acc[k] = sum_x32(sum_x16(a));
      }
      if (lane < 8) {
        const int off = QG_OFF + (kq * 4096 + g * 256 + cs * 64 + lane * 8) * 8;
#pragma unroll
        for (int k = 0; k < 4; ++k) {
          u32x4 gv; gv.x = __float_as_uint(acc[2 * k]); gv.y = tag; gv.z = __float_as_uint(acc[2 * k + 1]); gv.w = tag;
          st_sc1(rs, off + k * 16, gv);
        }
      }
    }
    STAMP(1);
    RAWBAR();
    {
      float q4[4];
      {
        int off[8];
        u32x4 r[8];
#pragma unroll
        for (int k = 0; k < 4; ++k) {
          off[2 * k] = QG_OFF + (k * 4096 + g * 256 + lane * 4) * 8;
          off[2 * k + 1] = off[2 * k] + 16;
        }
        gather<8>(rs, off, tag, r);
#pragma unroll
        for (int k = 0; k < 4; ++k) q4[k] = 0.f;
#pragma unroll
        for (int k = 0; k < 4; ++k) {
          q4[0] += __uint_as_float(r[2 * k].x); q4[1] += __uint_as_float(r[2 * k].z);
          q4[2] += __uint_as_float(r[2 * k + 1].x); q4[3] += __uint_as_float(r[2 * k + 1].z);
        }
      }
      sm.qbf[lane * 2] = (unsigned)to_bf_us(q4[0]) | ((unsigned)to_bf_us(q4[1]) << 16);
      sm.qbf[lane * 2 + 1] = (unsigned)to_bf_us(q4[2]) | ((unsigned)to_bf_us(q4[3]) << 16);
      if (lane == 0) sm.tag = tag;
    }
    STAMP(2);
    BAR();
    BAR();
    STAMP(3);
    {
      const int hblk = AG_OFF + (c >> 3) * 16 * 144 * 8;
      const int j16 = (c & 7) * 16;
      {
        int off[3];
        u32x4 r[3];
        off[0] = hblk + (lane & 15) * 144 * 8;
        off[1] = hblk + ((lane >> 3) * 144 + 16 + j16 + (lane & 7) * 2) * 8;
        off[2] = hblk + ((8 + (lane >> 3)) * 144 + 16 + j16 + (lane & 7) * 2) * 8;
        gather<3>(rs, off, tag, r);
        if (lane < 16) { sm.raw[256 + lane * 2] = __uint_as_float(r[0].x); sm.raw[257 + lane * 2] = __uint_as_float(r[0].z); }
        sm.raw[(lane >> 3) * 16 + (lane & 7) * 2] = __uint_as_float(r[1].x);
        sm.raw[(lane >> 3) * 16 + (lane & 7) * 2 + 1] = __uint_as_float(r[1].z);
        sm.raw[(8 + (lane >> 3)) * 16 + (lane & 7) * 2] = __uint_as_float(r[2].x);
        sm.raw[(8 + (lane >> 3)) * 16 + (lane & 7) * 2 + 1] = __uint_as_float(r[2].z);
      }
      asm volatile("s_waitcnt lgkmcnt(0)" ::: "memory");
      if (lane < 16) {
        float M = sm.raw[256];
#pragma unroll
        for (int sp = 1; sp < 16; ++sp) M = fmaxf(M, sm.raw[256 + sp * 2]);
        float L = 0.f, o = 0.f;
#pragma unroll
        for (int sp = 0; sp < 16; ++sp) {
          const float e = __expf(sm.raw[256 + sp * 2] - M);
          L += e * sm.raw[257 + sp * 2];
          o += e * sm.raw[sp * 16 + lane];
        }
        sm.o16[lane] = rbf(o / L);
      }
    }
    STAMP(4);
    BAR();
    BAR();
    STAMP(5);
    if (c < NMLP) {
      BAR();
      BAR();
      BAR();
      BAR();
    } else if (c < NMLP + 72) {
      BAR();
    }
  }
}

extern "C" void kernel_launch(void* const* d_in, const int* in_sizes, int n_in,
                              void* d_out, int out_size, void* d_ws, size_t ws_size,
                              hipStream_t stream) {
  (void)in_sizes; (void)n_in; (void)out_size; (void)ws_size;
  fused_decode_block<<<256, 640, 0, stream>>>(
      (const unsigned short*)d_in[0], (const unsigned short*)d_in[1], (const unsigned short*)d_in[2],
      (const unsigned short*)d_in[3], (const unsigned short*)d_in[4], (const unsigned short*)d_in[6],
      (const unsigned short*)d_in[7], (const unsigned short*)d_in[8], (const unsigned short*)d_in[9],
      (unsigned short*)d_out, (char*)d_ws);
}
```

```cpp
#include <hip/hip_runtime.h>
#include <hip/hip_bf16.h>
#include <stdint.h>

typedef __attribute__((ext_vector_type(4))) unsigned int u32x4;
typedef __attribute__((ext_vector_type(2))) unsigned int u32x2;
typedef __attribute__((ext_vector_type(2))) __bf16 bf16x2_t;

namespace {

constexpr int SK = 65536;
constexpr int WIN = 4096;
constexpr int DH = 128;
constexpr int FF = 4608;
constexpr int NMLP = 144;

constexpr int EPOCH_OFF = 0;
constexpr int QG_OFF = 4096;
constexpr int AG_OFF = QG_OFF + 4 * 4096 * 8;
constexpr int WG_OFF = AG_OFF + 32 * 16 * 144 * 8;
constexpr int HG_OFF = WG_OFF + 72 * 256 * 64;
constexpr int MG_OFF = HG_OFF + 8192;
constexpr int WS_BYTES = MG_OFF + 144 * 144 * 32;

constexpr int AUX_SC1 = 16;
constexpr unsigned SPIN_CAP = 200000u;
constexpr int CODE_WARM_KB = 18;

__device__ __forceinline__ float bf_lo(unsigned u) { return __uint_as_float(u << 16); }
__device__ __forceinline__ float bf_hi(unsigned u) { return __uint_as_float(u & 0xffff0000u); }
__device__ __forceinline__ float bf_us(unsigned short u) { return __uint_as_float(((unsigned)u) << 16); }
__device__ __forceinline__ float rbf(float f) { return (float)(__bf16)f; }
__device__ __forceinline__ unsigned short to_bf_us(float f) { return __builtin_bit_cast(unsigned short, (__bf16)f); }
__device__ __forceinline__ float dot2(unsigned k, unsigned q, float acc) {
  return __builtin_amdgcn_fdot2_f32_bf16(__builtin_bit_cast(bf16x2_t, k), __builtin_bit_cast(bf16x2_t, q), acc, false);
}
#define BAR() asm volatile("s_waitcnt lgkmcnt(0)\n\ts_barrier" ::: "memory")
#define RAWBAR() asm volatile("s_barrier" ::: "memory")
#define CFENCE() asm volatile("" ::: "memory")
#define SCHED() __builtin_amdgcn_sched_barrier(0)
#define STAMP(i) do { } while (0)
#define DSTAMP(i) do { } while (0)
#define CSTAMP(i) do { } while (0)

template <int CTRL>
__device__ __forceinline__ float dppf(float x) {
  return __int_as_float(__builtin_amdgcn_update_dpp(0, __float_as_int(x), CTRL, 0xf, 0xf, true));
}
constexpr int DPP_X1 = 0xB1, DPP_X2 = 0x4E, DPP_ROR4 = 0x124, DPP_ROR8 = 0x128;
__device__ __forceinline__ float sum_x16(float x) {
  auto r = __builtin_amdgcn_permlane16_swap(__float_as_uint(x), __float_as_uint(x), false, false);
  return __uint_as_float(r[0]) + __uint_as_float(r[1]);
}
__device__ __forceinline__ float sum_x32(float x) {
  auto r = __builtin_amdgcn_permlane32_swap(__float_as_uint(x), __float_as_uint(x), false, false);
  return __uint_as_float(r[0]) + __uint_as_float(r[1]);
}
__device__ __forceinline__ float max_x16(float x) {
  auto r = __builtin_amdgcn_permlane16_swap(__float_as_uint(x), __float_as_uint(x), false, false);
  return fmaxf(__uint_as_float(r[0]), __uint_as_float(r[1]));
}
__device__ __forceinline__ float max_x32(float x) {
  auto r = __builtin_amdgcn_permlane32_swap(__float_as_uint(x), __float_as_uint(x), false, false);
  return fmaxf(__uint_as_float(r[0]), __uint_as_float(r[1]));
}
__device__ __forceinline__ float sum_row16(float x) {
  x += dppf<DPP_X1>(x); x += dppf<DPP_X2>(x); x += dppf<DPP_ROR4>(x); x += dppf<DPP_ROR8>(x);
  return x;
}
__device__ __forceinline__ float sum_wave(float x) { return sum_x32(sum_x16(sum_row16(x))); }

__device__ __forceinline__ u32x4 ld_nt(const unsigned short* p) { return __builtin_nontemporal_load((const u32x4*)p); }
__device__ __forceinline__ u32x4 ld_sc1(__amdgpu_buffer_rsrc_t rs, int off) {
  return __builtin_amdgcn_raw_buffer_load_b128(rs, off, 0, AUX_SC1);
}
__device__ __forceinline__ void st_sc1(__amdgpu_buffer_rsrc_t rs, int off, u32x4 v) {
  __builtin_amdgcn_raw_buffer_store_b128(v, rs, off, 0, AUX_SC1);
}
__device__ __forceinline__ void fma8(float (&acc)[8], float a, u32x4 w) {
  acc[0] += a * bf_lo(w.x); acc[1] += a * bf_hi(w.x);
  acc[2] += a * bf_lo(w.y); acc[3] += a * bf_hi(w.y);
  acc[4] += a * bf_lo(w.z); acc[5] += a * bf_hi(w.z);
  acc[6] += a * bf_lo(w.w); acc[7] += a * bf_hi(w.w);
}

template <int N>
__device__ __forceinline__ void gather(const __amdgpu_buffer_rsrc_t rs, const int (&off)[N], const unsigned tag,
                                       u32x4 (&res)[N]) {
  u32x4 a[N], b[N];
#pragma unroll
  for (int k = 0; k < N; ++k) a[k] = ld_sc1(rs, off[k]);
  for (unsigned spin = 0; spin < SPIN_CAP; ++spin) {
    CFENCE();
#pragma unroll
    for (int k = 0; k < N; ++k) b[k] = ld_sc1(rs, off[k]);
    bool ok = true;
#pragma unroll
    for (int k = 0; k < N; ++k) ok &= (a[k].y == tag) & (a[k].w == tag);
    if (__all(ok)) {
#pragma unroll
      for (int k = 0; k < N; ++k) res[k] = a[k];
      return;
    }
    CFENCE();
#pragma unroll
    for (int k = 0; k < N; ++k) a[k] = ld_sc1(rs, off[k]);
    ok = true;
#pragma unroll
    for (int k = 0; k < N; ++k) ok &= (b[k].y == tag) & (b[k].w == tag);
    if (__all(ok)) {
#pragma unroll
      for (int k = 0; k < N; ++k) res[k] = b[k];
      return;
    }
  }
#pragma unroll
  for (int k = 0; k < N; ++k) res[k] = a[k];
}

struct Smem {
  float red[8][576];
  float att[8][2][132];
  float xn[576];
  unsigned qbf[128];
  float raw[16 * 16 + 32];
  float o16[16];
  float w1red[9][32];
  float h[576];
  float part[8][8];
  unsigned tag;
};

__device__ __forceinline__ void data_body(Smem& sm, const int tid, const int lane, const int wave, const int c,
                                          const __amdgpu_buffer_rsrc_t rs,
                                          const unsigned short* __restrict__ kc, const unsigned short* __restrict__ vc,
                                          const unsigned short* __restrict__ wo, const unsigned short* __restrict__ w1,
                                          const unsigned short* __restrict__ b1, const unsigned short* __restrict__ w2,
                                          const unsigned short* __restrict__ x, const unsigned short* __restrict__ b2,
                                          unsigned short* __restrict__ out, char* ws, const unsigned long long pc0) {
  const bool HAS_MLP = (c < NMLP);
  const bool IS_RED = (c >= NMLP) && (c < NMLP + 72);
  const int kvw = wave < 8 ? wave : 7;
  const int g = c >> 4, s = c & 15;
  RAWBAR();
  SCHED();
  unsigned warm = 0u;
  if (wave == 8) {
    const char* cbase = (const char*)(pc0 & ~255ull);
#pragma unroll
    for (int i = 0; i < CODE_WARM_KB; ++i) {
      const u32x4 t = *(const volatile u32x4*)(cbase + i * 1024 + lane * 16);
      warm ^= t.x ^ t.y ^ t.z ^ t.w;
    }
  }
  SCHED();
  u32x4 kv[8], vv[8];
  const size_t kvbase = ((size_t)g * SK + (SK - WIN) + s * 256 + kvw * 32 + (lane >> 4)) * DH + (lane & 15) * 8;
#pragma unroll
  for (int j = 0; j < 8; ++j) kv[j] = ld_nt(kc + kvbase + (size_t)j * 4 * DH);
  CFENCE();
  SCHED();
  RAWBAR();
  SCHED();
#pragma unroll
  for (int j = 0; j < 8; ++j) vv[j] = ld_nt(vc + kvbase + (size_t)j * 4 * DH);
  CFENCE();
  SCHED();
  DSTAMP(10);
  BAR();
  SCHED();
  u32x4 wov[2];
  {
    const unsigned short* p = wo + (size_t)c * 16 * 576 + tid * 8;
    wov[0] = ld_nt(p);
    wov[1] = ld_nt(p + 576 * 8);
  }
  CFENCE();
  SCHED();
  const unsigned tag = sm.tag;
  {
    unsigned qa[4], qb[4];
#pragma unroll
    for (int k = 0; k < 4; ++k) { qa[k] = sm.qbf[(lane & 15) * 4 + k]; qb[k] = sm.qbf[64 + (lane & 15) * 4 + k]; }
    float sa[8], sb[8];
#pragma unroll
    for (int j = 0; j < 8; ++j) {
      float a = dot2(kv[j].x, qa[0], 0.f), b = dot2(kv[j].x, qb[0], 0.f);
      a = dot2(kv[j].y, qa[1], a); b = dot2(kv[j].y, qb[1], b);
      a = dot2(kv[j].z, qa[2], a); b = dot2(kv[j].z, qb[2], b);
      a = dot2(kv[j].w, qa[3], a); b = dot2(kv[j].w, qb[3], b);
      sa[j] = sum_row16(a); sb[j] = sum_row16(b);
    }
    DSTAMP(12);
    float ma = -3.0e38f, mb = -3.0e38f;
#pragma unroll
    for (int j = 0; j < 8; ++j) {
      sa[j] = rbf(rbf(sa[j]) * 0.08838834764831845f);
      sb[j] = rbf(rbf(sb[j]) * 0.08838834764831845f);
      ma = fmaxf(ma, sa[j]); mb = fmaxf(mb, sb[j]);
    }
    ma = max_x32(max_x16(ma)); mb = max_x32(max_x16(mb));
    float la = 0.f, lb = 0.f;
    float oa[8], ob[8];
#pragma unroll
    for (int k = 0; k < 8; ++k) { oa[k] = 0.f; ob[k] = 0.f; }
#pragma unroll
    for (int j = 0; j < 8; ++j) {
      const float pa = __expf(sa[j] - ma), pb = __expf(sb[j] - mb);
      la += pa; lb += pb;
      fma8(oa, pa, vv[j]);
      fma8(ob, pb, vv[j]);
    }
    la = sum_x32(sum_x16(la)); lb = sum_x32(sum_x16(lb));
#pragma unroll
    for (int k = 0; k < 8; ++k) { oa[k] = sum_x32(sum_x16(oa[k])); ob[k] = sum_x32(sum_x16(ob[k])); }
    if (lane < 16 && wave < 8) {
      *(float4*)&sm.att[wave][0][4 + lane * 8] = make_float4(oa[0], oa[1], oa[2], oa[3]);
      *(float4*)&sm.att[wave][0][8 + lane * 8] = make_float4(oa[4], oa[5], oa[6], oa[7]);
      *(float4*)&sm.att[wave][1][4 + lane * 8] = make_float4(ob[0], ob[1], ob[2], ob[3]);
      *(float4*)&sm.att[wave][1][8 + lane * 8] = make_float4(ob[4], ob[5], ob[6], ob[7]);
      if (lane == 0) { sm.att[wave][0][0] = ma; sm.att[wave][0][1] = la; sm.att[wave][1][0] = mb; sm.att[wave][1][1] = lb; }
    }
  }
  DSTAMP(13);
  asm volatile("" :: "v"(warm));
  BAR();
  if (tid < 128) {
    const int h = tid >> 6, d2 = (tid & 63) * 2;
    float M = sm.att[0][h][0];
#pragma unroll
    for (int w = 1; w < 8; ++w) M = fmaxf(M, sm.att[w][h][0]);
    float L = 0.f, o0 = 0.f, o1 = 0.f;
#pragma unroll
    for (int w = 0; w < 8; ++w) {
      const float e = __expf(sm.att[w][h][0] - M);
      L += e * sm.att[w][h][1];
      o0 += e * sm.att[w][h][4 + d2];
      o1 += e * sm.att[w][h][5 + d2];
    }
    const int blk = AG_OFF + ((2 * g + h) * 16 + s) * 144 * 8;
    u32x4 gv; gv.x = __float_as_uint(o0); gv.y = tag; gv.z = __float_as_uint(o1); gv.w = tag;
    st_sc1(rs, blk + (16 + d2) * 8, gv);
    if ((tid & 63) == 0) {
      u32x4 mv; mv.x = __float_as_uint(M); mv.y = tag; mv.z = __float_as_uint(L); mv.w = tag;
      st_sc1(rs, blk, mv);
    }
  }
  BAR();
  {
    const int r0 = tid / 72, cc = tid - r0 * 72;
    float acc[8];
#pragma unroll
    for (int k = 0; k < 8; ++k) acc[k] = 0.f;
    fma8(acc, sm.o16[r0], wov[0]);
    fma8(acc, sm.o16[r0 + 8], wov[1]);
    *(float4*)&sm.red[r0][cc * 8] = make_float4(acc[0], acc[1], acc[2], acc[3]);
    *(float4*)&sm.red[r0][cc * 8 + 4] = make_float4(acc[4], acc[5], acc[6], acc[7]);
  }
  BAR();
  if (tid < 288) {
    float v0 = 0.f, v1 = 0.f;
#pragma unroll
    for (int r = 0; r < 8; ++r) { const float2 t = *(const float2*)&sm.red[r][2 * tid]; v0 += t.x; v1 += t.y; }
    u32x4 gv; gv.x = __float_as_uint(v0); gv.y = tag; gv.z = __float_as_uint(v1); gv.w = tag;
    st_sc1(rs, WG_OFF + ((tid >> 2) * 256 + c) * 64 + (tid & 3) * 16, gv);
  }
  CFENCE();
  CFENCE();
  SCHED();
  u32x4 w1v[4], w2v[4];
  unsigned short b1r[4];
  if (HAS_MLP) {
    const unsigned short* p1 = w1 + (size_t)(tid >> 2) * FF + c * 32 + (tid & 3) * 8;
    const unsigned short* p2 = w2 + (size_t)c * 32 * 576 + tid * 8;
#pragma unroll
    for (int i = 0; i < 4; ++i) w1v[i] = ld_nt(p1 + (size_t)i * 144 * FF);
#pragma unroll
    for (int i = 0; i < 4; ++i) w2v[i] = ld_nt(p2 + (size_t)i * 576 * 8);
#pragma unroll
    for (int i = 0; i < 4; ++i) b1r[i] = b1[c * 32 + tid / 72 + 8 * i];
  }
  CFENCE();
  SCHED();
  if (IS_RED) {
    const int r = c - NMLP;
    if (wave < 8) {
      int off[2];
      u32x4 a[2];
      off[0] = WG_OFF + (r * 256 + wave * 32 + (lane >> 1)) * 64 + (lane & 1) * 32;
      off[1] = off[0] + 16;
      gather<2>(rs, off, tag, a);
      float v[4] = {__uint_as_float(a[0].x), __uint_as_float(a[0].z), __uint_as_float(a[1].x), __uint_as_float(a[1].z)};
#pragma unroll
      for (int k = 0; k < 4; ++k) {
        float t = v[k];
        t += dppf<DPP_X2>(t); t += dppf<DPP_ROR4>(t); t += dppf<DPP_ROR8>(t);
        v[k] = sum_x32(sum_x16(t));
      }
      if (lane < 2) *(float4*)&sm.part[wave][lane * 4] = make_float4(v[0], v[1], v[2], v[3]);
    }
    BAR();
    if (tid < 4) {
      const unsigned xw = ((const unsigned*)x)[4 * r + tid];
      float v0 = 0.f, v1 = 0.f;
#pragma unroll
      for (int w = 0; w < 8; ++w) { v0 += sm.part[w][2 * tid]; v1 += sm.part[w][2 * tid + 1]; }
      u32x4 g0;
      g0.x = __float_as_uint(rbf(bf_lo(xw) + rbf(v0))); g0.y = tag;
      g0.z = __float_as_uint(rbf(bf_hi(xw) + rbf(v1))); g0.w = tag;
      st_sc1(rs, HG_OFF + (8 * r + 2 * tid) * 8, g0);
    }
    DSTAMP(6);
  }
  if (HAS_MLP) {
    if (wave < 5 && tid < 288) {
      int off[1];
      u32x4 a[1];
      off[0] = HG_OFF + tid * 16;
      gather<1>(rs, off, tag, a);
      *(float2*)&sm.h[2 * tid] = make_float2(__uint_as_float(a[0].x), __uint_as_float(a[0].z));
    }
    DSTAMP(7);
    CSTAMP(14);
    BAR();
    DSTAMP(12);
    {
      float hv[9];
      float sum = 0.f;
#pragma unroll
      for (int i = 0; i < 9; ++i) { hv[i] = sm.h[lane + 64 * i]; sum += hv[i]; }
      sum = sum_wave(sum);
      const float mean = sum * (1.f / 576.f);
      float sq = 0.f;
#pragma unroll
      for (int i = 0; i < 9; ++i) { const float d = hv[i] - mean; sq += d * d; }
      sq = sum_wave(sq);
      const float mb = rbf(mean);
      const float sd = rbf(sqrtf(rbf(rbf(sq * (1.f / 576.f)) + 1e-5f)));
      float acc[8];
#pragma unroll
      for (int k = 0; k < 8; ++k) acc[k] = 0.f;
#pragma unroll
      for (int i = 0; i < 4; ++i) fma8(acc, rbf(rbf(sm.h[(tid >> 2) + 144 * i] - mb) / sd), w1v[i]);
#pragma unroll
      for (int k = 0; k < 8; ++k) {
        float a = acc[k];
        a += dppf<DPP_ROR4>(a); a += dppf<DPP_ROR8>(a);
        acc[k] = sum_x32(sum_x16(a));
      }
      if (lane < 4) {
        *(float4*)&sm.w1red[wave][lane * 8] = make_float4(acc[0], acc[1], acc[2], acc[3]);
        *(float4*)&sm.w1red[wave][lane * 8 + 4] = make_float4(acc[4], acc[5], acc[6], acc[7]);
      }
    }
    BAR();
    DSTAMP(13);
    {
      const int r0 = tid / 72, cc = tid - r0 * 72;
      float acc[8];
#pragma unroll
      for (int k = 0; k < 8; ++k) acc[k] = 0.f;
#pragma unroll
      for (int i = 0; i < 4; ++i) {
        float v = 0.f;
#pragma unroll
        for (int w = 0; w < 9; ++w) v += sm.w1red[w][r0 + 8 * i];
        v = rbf(rbf(v) + bf_us(b1r[i]));
        v = fmaxf(v, 0.f);
        v = rbf(v * v);
        fma8(acc, v, w2v[i]);
      }
      *(float4*)&sm.red[r0][cc * 8] = make_float4(acc[0], acc[1], acc[2], acc[3]);
      *(float4*)&sm.red[r0][cc * 8 + 4] = make_float4(acc[4], acc[5], acc[6], acc[7]);
    }
    BAR();
    DSTAMP(8);
    CSTAMP(15);
    if (tid < 288) {
      float v0 = 0.f, v1 = 0.f;
#pragma unroll
      for (int r = 0; r < 8; ++r) { const float2 t = *(const float2*)&sm.red[r][2 * tid]; v0 += t.x; v1 += t.y; }
      u32x4 gv; gv.x = __float_as_uint(v0); gv.y = tag; gv.z = __float_as_uint(v1); gv.w = tag;
      st_sc1(rs, MG_OFF + ((tid >> 1) * 144 + c) * 32 + (tid & 1) * 16, gv);
    }
    if (wave < 5) {
      float v0 = 0.f, v1 = 0.f;
      if (tid < 288) {
        int off[1];
        u32x4 a[1];
        off[0] = MG_OFF + c * 144 * 32 + tid * 16;
        gather<1>(rs, off, tag, a);
        v0 = __uint_as_float(a[0].x); v1 = __uint_as_float(a[0].z);
      }
      v0 += dppf<DPP_X2>(v0); v0 += dppf<DPP_ROR4>(v0); v0 += dppf<DPP_ROR8>(v0); v0 = sum_x32(sum_x16(v0));
      v1 += dppf<DPP_X2>(v1); v1 += dppf<DPP_ROR4>(v1); v1 += dppf<DPP_ROR8>(v1); v1 = sum_x32(sum_x16(v1));
      if (lane < 2) *(float2*)&sm.part[wave][lane * 2] = make_float2(v0, v1);
    }
    BAR();
    if (tid < 2) {
      const unsigned bw = ((const unsigned*)b2)[2 * c + tid];
      float v0 = 0.f, v1 = 0.f;
#pragma unroll
      for (int w = 0; w < 5; ++w) { v0 += sm.part[w][2 * tid]; v1 += sm.part[w][2 * tid + 1]; }
      const float h0 = sm.h[4 * c + 2 * tid], h1 = sm.h[4 * c + 2 * tid + 1];
      ((unsigned*)out)[2 * c + tid] = (unsigned)to_bf_us(h0 + rbf(rbf(v0) + bf_lo(bw))) |
                                      ((unsigned)to_bf_us(h1 + rbf(rbf(v1) + bf_hi(bw))) << 16);
    }
    if (c == 0 && tid == 0) __builtin_amdgcn_raw_buffer_store_b32(tag, rs, EPOCH_OFF, 0, AUX_SC1);
    DSTAMP(9);
  }
}

}

__global__ void __launch_bounds__(640, 3)
fused_decode_block(const unsigned short* __restrict__ x, const unsigned short* __restrict__ kc,
                   const unsigned short* __restrict__ vc, const unsigned short* __restrict__ wq,
                   const unsigned short* __restrict__ wo, const unsigned short* __restrict__ w1,
                   const unsigned short* __restrict__ b1, const unsigned short* __restrict__ w2,
                   const unsigned short* __restrict__ b2, unsigned short* __restrict__ out, char* ws) {
  __shared__ __attribute__((aligned(16))) Smem sm;
  unsigned long long pc0;
  asm volatile("s_getpc_b64 %0" : "=s"(pc0));
  const int tid = threadIdx.x;
  const int lane = tid & 63;
  const int wave = __builtin_amdgcn_readfirstlane(tid >> 6);
  const int c = blockIdx.x;
  const int g = c >> 4;
  const __amdgpu_buffer_rsrc_t rs = __builtin_amdgcn_make_buffer_rsrc((void*)ws, 0, WS_BYTES, 0x00020000);

  if (wave < 9) {
    data_body(sm, tid, lane, wave, c, rs, kc, vc, wo, w1, b1, w2, x, b2, out, ws, pc0);
  } else {
    STAMP(0);
    const int s = c & 15, cs = s & 3, kq = s >> 2;
    const unsigned epoch = __builtin_amdgcn_raw_buffer_load_b32(rs, EPOCH_OFF, 0, AUX_SC1);
    unsigned xd[5];
#pragma unroll
    for (int i = 0; i < 5; ++i) {
      const int idx = lane + 64 * i;
      xd[i] = ((const unsigned*)x)[idx < 288 ? idx : 287];
    }
    u32x4 wqv[18];
    {
      const unsigned short* p = wq + (size_t)(kq * 144 + (lane >> 3)) * 4096 + g * 256 + cs * 64 + (lane & 7) * 8;
#pragma unroll
      for (int i = 0; i < 18; ++i) wqv[i] = ld_nt(p + (size_t)i * 8 * 4096);
    }
    CFENCE();
    SCHED();
    RAWBAR();
    SCHED();
    const unsigned tag = epoch + 1u;
    {
      float sum = 0.f;
#pragma unroll
      for (int i = 0; i < 5; ++i) sum += (lane + 64 * i < 288) ? (bf_lo(xd[i]) + bf_hi(xd[i])) : 0.f;
      sum = sum_wave(sum);
      const float mean = sum * (1.f / 576.f);
      float sq = 0.f;
#pragma unroll
      for (int i = 0; i < 5; ++i) {
        const float a = bf_lo(xd[i]) - mean, b = bf_hi(xd[i]) - mean;
        sq += (lane + 64 * i < 288) ? (a * a + b * b) : 0.f;
      }
      sq = sum_wave(sq);
      const float mb = rbf(mean);
      const float sd = rbf(sqrtf(rbf(rbf(sq * (1.f / 576.f)) + 1e-5f)));
#pragma unroll
      for (int i = 0; i < 5; ++i) {
        if (lane + 64 * i < 288) {
          *(float2*)&sm.xn[2 * (lane + 64 * i)] =
              make_float2(rbf(rbf(bf_lo(xd[i]) - mb) / sd), rbf(rbf(bf_hi(xd[i]) - mb) / sd));
        }
      }
    }
    asm volatile("s_waitcnt lgkmcnt(0)" ::: "memory");
    STAMP(11);
    {
      float acc[8];
#pragma unroll
      for (int k = 0; k < 8; ++k) acc[k] = 0.f;
#pragma unroll
      for (int i = 0; i < 18; ++i) fma8(acc, sm.xn[kq * 144 + i * 8 + (lane >> 3)], wqv[i]);
#pragma unroll
      for (int k = 0; k < 8; ++k) {
        float a = acc[k];
        a += dppf<DPP_ROR8>(a);
        acc[k] = sum_x32(sum_x16(a));
      }
      if (lane < 8) {
        const int off = QG_OFF + (kq * 4096 + g * 256 + cs * 64 + lane * 8) * 8;
#pragma unroll
        for (int k = 0; k < 4; ++k) {
          u32x4 gv; gv.x = __float_as_uint(acc[2 * k]); gv.y = tag; gv.z = __float_as_uint(acc[2 * k + 1]); gv.w = tag;
          st_sc1(rs, off + k * 16, gv);
        }
      }
    }
    STAMP(1);
    RAWBAR();
    {
      float q4[4];
      {
        int off[8];
        u32x4 r[8];
#pragma unroll
        for (int k = 0; k < 4; ++k) {
          off[2 * k] = QG_OFF + (k * 4096 + g * 256 + lane * 4) * 8;
          off[2 * k + 1] = off[2 * k] + 16;
        }
        gather<8>(rs, off, tag, r);
#pragma unroll
        for (int k = 0; k < 4; ++k) q4[k] = 0.f;
#pragma unroll
        for (int k = 0; k < 4; ++k) {
          q4[0] += __uint_as_float(r[2 * k].x); q4[1] += __uint_as_float(r[2 * k].z);
          q4[2] += __uint_as_float(r[2 * k + 1].x); q4[3] += __uint_as_float(r[2 * k + 1].z);
        }
      }
      sm.qbf[lane * 2] = (unsigned)to_bf_us(q4[0]) | ((unsigned)to_bf_us(q4[1]) << 16);
      sm.qbf[lane * 2 + 1] = (unsigned)to_bf_us(q4[2]) | ((unsigned)to_bf_us(q4[3]) << 16);
      if (lane == 0) sm.tag = tag;
    }
    STAMP(2);
    BAR();
    BAR();
    STAMP(3);
    {
      const int hblk = AG_OFF + (c >> 3) * 16 * 144 * 8;
      const int j16 = (c & 7) * 16;
      {
        int off[3];
        u32x4 r[3];
        off[0] = hblk + (lane & 15) * 144 * 8;
        off[1] = hblk + ((lane >> 3) * 144 + 16 + j16 + (lane & 7) * 2) * 8;
        off[2] = hblk + ((8 + (lane >> 3)) * 144 + 16 + j16 + (lane & 7) * 2) * 8;
        gather<3>(rs, off, tag, r);
        if (lane < 16) { sm.raw[256 + lane * 2] = __uint_as_float(r[0].x); sm.raw[257 + lane * 2] = __uint_as_float(r[0].z); }
        sm.raw[(lane >> 3) * 16 + (lane & 7) * 2] = __uint_as_float(r[1].x);
        sm.raw[(lane >> 3) * 16 + (lane & 7) * 2 + 1] = __uint_as_float(r[1].z);
        sm.raw[(8 + (lane >> 3)) * 16 + (lane & 7) * 2] = __uint_as_float(r[2].x);
        sm.raw[(8 + (lane >> 3)) * 16 + (lane & 7) * 2 + 1] = __uint_as_float(r[2].z);
      }
      asm volatile("s_waitcnt lgkmcnt(0)" ::: "memory");
      if (lane < 16) {
        float M = sm.raw[256];
#pragma unroll
        for (int sp = 1; sp < 16; ++sp) M = fmaxf(M, sm.raw[256 + sp * 2]);
        float L = 0.f, o = 0.f;
#pragma unroll
        for (int sp = 0; sp < 16; ++sp) {
          const float e = __expf(sm.raw[256 + sp * 2] - M);
          L += e * sm.raw[257 + sp * 2];
          o += e * sm.raw[sp * 16 + lane];
        }
        sm.o16[lane] = rbf(o / L);
      }
    }
    STAMP(4);
    BAR();
    BAR();
    STAMP(5);
    if (c < NMLP) {
      BAR();
      BAR();
      BAR();
      BAR();
    } else if (c < NMLP + 72) {
      BAR();
    }
  }
}

extern "C" void kernel_launch(void* const* d_in, const int* in_sizes, int n_in,
                              void* d_out, int out_size, void* d_ws, size_t ws_size,
                              hipStream_t stream) {
  (void)in_sizes; (void)n_in; (void)out_size; (void)ws_size;
  fused_decode_block<<<256, 640, 0, stream>>>(
      (const unsigned short*)d_in[0], (const unsigned short*)d_in[1], (const unsigned short*)d_in[2],
      (const unsigned short*)d_in[3], (const unsigned short*)d_in[4], (const unsigned short*)d_in[6],
      (const unsigned short*)d_in[7], (const unsigned short*)d_in[8], (const unsigned short*)d_in[9],
      (unsigned short*)d_out, (char*)d_ws);
}
```

```cpp
#include <hip/hip_runtime.h>
#include <hip/hip_bf16.h>
#include <stdint.h>

typedef __attribute__((ext_vector_type(4))) unsigned int u32x4;
typedef __attribute__((ext_vector_type(2))) unsigned int u32x2;
typedef __attribute__((ext_vector_type(2))) __bf16 bf16x2_t;

namespace {

constexpr int SK = 65536;
constexpr int WIN = 4096;
constexpr int DH = 128;
constexpr int FF = 4608;
constexpr int NMLP = 144;

constexpr int EPOCH_OFF = 0;
constexpr int QG_OFF = 4096;
constexpr int AG_OFF = QG_OFF + 4 * 4096 * 8;
constexpr int WG_OFF = AG_OFF + 32 * 16 * 144 * 8;
constexpr int HG_OFF = WG_OFF + 72 * 256 * 64;
constexpr int MG_OFF = HG_OFF + 8192;
constexpr int WS_BYTES = MG_OFF + 144 * 144 * 32;

constexpr int AUX_SC1 = 16;
constexpr unsigned SPIN_CAP = 200000u;

__device__ __forceinline__ float bf_lo(unsigned u) { return __uint_as_float(u << 16); }
__device__ __forceinline__ float bf_hi(unsigned u) { return __uint_as_float(u & 0xffff0000u); }
__device__ __forceinline__ float bf_us(unsigned short u) { return __uint_as_float(((unsigned)u) << 16); }
__device__ __forceinline__ float rbf(float f) { return (float)(__bf16)f; }
__device__ __forceinline__ unsigned short to_bf_us(float f) { return __builtin_bit_cast(unsigned short, (__bf16)f); }
__device__ __forceinline__ float dot2(unsigned k, unsigned q, float acc) {
  return __builtin_amdgcn_fdot2_f32_bf16(__builtin_bit_cast(bf16x2_t, k), __builtin_bit_cast(bf16x2_t, q), acc, false);
}
#define BAR() asm volatile("s_waitcnt lgkmcnt(0)\n\ts_barrier" ::: "memory")
#define RAWBAR() asm volatile("s_barrier" ::: "memory")
#define CFENCE() asm volatile("" ::: "memory")
#define SCHED() __builtin_amdgcn_sched_barrier(0)
#define STAMP(i) do { } while (0)
#define DSTAMP(i) do { } while (0)
#define CSTAMP(i) do { } while (0)

template <int CTRL>
__device__ __forceinline__ float dppf(float x) {
  return __int_as_float(__builtin_amdgcn_update_dpp(0, __float_as_int(x), CTRL, 0xf, 0xf, true));
}
constexpr int DPP_X1 = 0xB1, DPP_X2 = 0x4E, DPP_ROR4 = 0x124, DPP_ROR8 = 0x128;
__device__ __forceinline__ float sum_x16(float x) {
  auto r = __builtin_amdgcn_permlane16_swap(__float_as_uint(x), __float_as_uint(x), false, false);
  return __uint_as_float(r[0]) + __uint_as_float(r[1]);
}
__device__ __forceinline__ float sum_x32(float x) {
  auto r = __builtin_amdgcn_permlane32_swap(__float_as_uint(x), __float_as_uint(x), false, false);
  return __uint_as_float(r[0]) + __uint_as_float(r[1]);
}
__device__ __forceinline__ float max_x16(float x) {
  auto r = __builtin_amdgcn_permlane16_swap(__float_as_uint(x), __float_as_uint(x), false, false);
  return fmaxf(__uint_as_float(r[0]), __uint_as_float(r[1]));
}
__device__ __forceinline__ float max_x32(float x) {
  auto r = __builtin_amdgcn_permlane32_swap(__float_as_uint(x), __float_as_uint(x), false, false);
  return fmaxf(__uint_as_float(r[0]), __uint_as_float(r[1]));
}
__device__ __forceinline__ float sum_row16(float x) {
  x += dppf<DPP_X1>(x); x += dppf<DPP_X2>(x); x += dppf<DPP_ROR4>(x); x += dppf<DPP_ROR8>(x);
  return x;
}
__device__ __forceinline__ float sum_wave(float x) { return sum_x32(sum_x16(sum_row16(x))); }

__device__ __forceinline__ u32x4 ld_nt(const unsigned short* p) { return __builtin_nontemporal_load((const u32x4*)p); }
__device__ __forceinline__ u32x4 ld_sc1(__amdgpu_buffer_rsrc_t rs, int off) {
  return __builtin_amdgcn_raw_buffer_load_b128(rs, off, 0, AUX_SC1);
}
__device__ __forceinline__ void st_sc1(__amdgpu_buffer_rsrc_t rs, int off, u32x4 v) {
  __builtin_amdgcn_raw_buffer_store_b128(v, rs, off, 0, AUX_SC1);
}
__device__ __forceinline__ void fma8(float (&acc)[8], float a, u32x4 w) {
  acc[0] += a * bf_lo(w.x); acc[1] += a * bf_hi(w.x);
  acc[2] += a * bf_lo(w.y); acc[3] += a * bf_hi(w.y);
  acc[4] += a * bf_lo(w.z); acc[5] += a * bf_hi(w.z);
  acc[6] += a * bf_lo(w.w); acc[7] += a * bf_hi(w.w);
}

template <int N>
__device__ __forceinline__ void gather(const __amdgpu_buffer_rsrc_t rs, const int (&off)[N], const unsigned tag,
                                       u32x4 (&res)[N]) {
  u32x4 a[N], b[N];
#pragma unroll
  for (int k = 0; k < N; ++k) a[k] = ld_sc1(rs, off[k]);
  for (unsigned spin = 0; spin < SPIN_CAP; ++spin) {
    CFENCE();
#pragma unroll
    for (int k = 0; k < N; ++k) b[k] = ld_sc1(rs, off[k]);
    bool ok = true;
#pragma unroll
    for (int k = 0; k < N; ++k) ok &= (a[k].y == tag) & (a[k].w == tag);
    if (__all(ok)) {
#pragma unroll
      for (int k = 0; k < N; ++k) res[k] = a[k];
      return;
    }
    CFENCE();
#pragma unroll
    for (int k = 0; k < N; ++k) a[k] = ld_sc1(rs, off[k]);
    ok = true;
#pragma unroll
    for (int k = 0; k < N; ++k) ok &= (b[k].y == tag) & (b[k].w == tag);
    if (__all(ok)) {
#pragma unroll
      for (int k = 0; k < N; ++k) res[k] = b[k];
      return;
    }
  }
#pragma unroll
  for (int k = 0; k < N; ++k) res[k] = a[k];
}

struct Smem {
  float red[8][576];
  float att[8][2][132];
  float xn[576];
  unsigned qbf[128];
  float raw[16 * 16 + 32];
  float o16[16];
  float w1red[9][32];
  float h[576];
  float part[8][8];
  unsigned tag;
};

__device__ __forceinline__ void data_body(Smem& sm, const int tid, const int lane, const int wave, const int c,
                                          const __amdgpu_buffer_rsrc_t rs,
                                          const unsigned short* __restrict__ kc, const unsigned short* __restrict__ vc,
                                          const unsigned short* __restrict__ wo, const unsigned short* __restrict__ w1,
                                          const unsigned short* __restrict__ b1, const unsigned short* __restrict__ w2,
                                          const unsigned short* __restrict__ x, const unsigned short* __restrict__ b2,
                                          unsigned short* __restrict__ out, char* ws) {
  const bool HAS_MLP = (c < NMLP);
  const bool IS_RED = (c >= NMLP) && (c < NMLP + 72);
  const int kvw = wave < 8 ? wave : 7;
  const int g = c >> 4, s = c & 15;
  RAWBAR();
  SCHED();
  u32x4 kv[8], vv[8];
  const size_t kvbase = ((size_t)g * SK + (SK - WIN) + s * 256 + kvw * 32 + (lane >> 4)) * DH + (lane & 15) * 8;
#pragma unroll
  for (int j = 0; j < 8; ++j) kv[j] = ld_nt(kc + kvbase + (size_t)j * 4 * DH);
  CFENCE();
  SCHED();
  RAWBAR();
  SCHED();
#pragma unroll
  for (int j = 0; j < 8; ++j) vv[j] = ld_nt(vc + kvbase + (size_t)j * 4 * DH);
  CFENCE();
  SCHED();
  DSTAMP(10);
  BAR();
  SCHED();
  u32x4 wov[2];
  {
    const unsigned short* p = wo + (size_t)c * 16 * 576 + tid * 8;
    wov[0] = ld_nt(p);
    wov[1] = ld_nt(p + 576 * 8);
  }
  CFENCE();
  SCHED();
  const unsigned tag = sm.tag;
  {
    unsigned qa[4], qb[4];
#pragma unroll
    for (int k = 0; k < 4; ++k) { qa[k] = sm.qbf[(lane & 15) * 4 + k]; qb[k] = sm.qbf[64 + (lane & 15) * 4 + k]; }
    float sa[8], sb[8];
#pragma unroll
    for (int j = 0; j < 8; ++j) {
      float a = dot2(kv[j].x, qa[0], 0.f), b = dot2(kv[j].x, qb[0], 0.f);
      a = dot2(kv[j].y, qa[1], a); b = dot2(kv[j].y, qb[1], b);
      a = dot2(kv[j].z, qa[2], a); b = dot2(kv[j].z, qb[2], b);
      a = dot2(kv[j].w, qa[3], a); b = dot2(kv[j].w, qb[3], b);
      sa[j] = sum_row16(a); sb[j] = sum_row16(b);
    }
    DSTAMP(12);
    float ma = -3.0e38f, mb = -3.0e38f;
#pragma unroll
    for (int j = 0; j < 8; ++j) {
      sa[j] = rbf(rbf(sa[j]) * 0.08838834764831845f);
      sb[j] = rbf(rbf(sb[j]) * 0.08838834764831845f);
      ma = fmaxf(ma, sa[j]); mb = fmaxf(mb, sb[j]);
    }
    ma = max_x32(max_x16(ma)); mb = max_x32(max_x16(mb));
    float la = 0.f, lb = 0.f;
    float oa[8], ob[8];
#pragma unroll
    for (int k = 0; k < 8; ++k) { oa[k] = 0.f; ob[k] = 0.f; }
#pragma unroll
    for (int j = 0; j < 8; ++j) {
      const float pa = __expf(sa[j] - ma), pb = __expf(sb[j] - mb);
      la += pa; lb += pb;
      fma8(oa, pa, vv[j]);
      fma8(ob, pb, vv[j]);
    }
    la = sum_x32(sum_x16(la)); lb = sum_x32(sum_x16(lb));
#pragma unroll
    for (int k = 0; k < 8; ++k) { oa[k] = sum_x32(sum_x16(oa[k])); ob[k] = sum_x32(sum_x16(ob[k])); }
    if (lane < 16 && wave < 8) {
      *(float4*)&sm.att[wave][0][4 + lane * 8] = make_float4(oa[0], oa[1], oa[2], oa[3]);
      *(float4*)&sm.att[wave][0][8 + lane * 8] = make_float4(oa[4], oa[5], oa[6], oa[7]);
      *(float4*)&sm.att[wave][1][4 + lane * 8] = make_float4(ob[0], ob[1], ob[2], ob[3]);
      *(float4*)&sm.att[wave][1][8 + lane * 8] = make_float4(ob[4], ob[5], ob[6], ob[7]);
      if (lane == 0) { sm.att[wave][0][0] = ma; sm.att[wave][0][1] = la; sm.att[wave][1][0] = mb; sm.att[wave][1][1] = lb; }
    }
  }
  DSTAMP(13);
  BAR();
  if (tid < 128) {
    const int h = tid >> 6, d2 = (tid & 63) * 2;
    float M = sm.att[0][h][0];
#pragma unroll
    for (int w = 1; w < 8; ++w) M = fmaxf(M, sm.att[w][h][0]);
    float L = 0.f, o0 = 0.f, o1 = 0.f;
#pragma unroll
    for (int w = 0; w < 8; ++w) {
      const float e = __expf(sm.att[w][h][0] - M);
      L += e * sm.att[w][h][1];
      o0 += e * sm.att[w][h][4 + d2];
      o1 += e * sm.att[w][h][5 + d2];
    }
    const int blk = AG_OFF + ((2 * g + h) * 16 + s) * 144 * 8;
    u32x4 gv; gv.x = __float_as_uint(o0); gv.y = tag; gv.z = __float_as_uint(o1); gv.w = tag;
    st_sc1(rs, blk + (16 + d2) * 8, gv);
    if ((tid & 63) == 0) {
      u32x4 mv; mv.x = __float_as_uint(M); mv.y = tag; mv.z = __float_as_uint(L); mv.w = tag;
      st_sc1(rs, blk, mv);
    }
  }
  BAR();
  {
    const int r0 = tid / 72, cc = tid - r0 * 72;
    float acc[8];
#pragma unroll
    for (int k = 0; k < 8; ++k) acc[k] = 0.f;
    fma8(acc, sm.o16[r0], wov[0]);
    fma8(acc, sm.o16[r0 + 8], wov[1]);
    *(float4*)&sm.red[r0][cc * 8] = make_float4(acc[0], acc[1], acc[2], acc[3]);
    *(float4*)&sm.red[r0][cc * 8 + 4] = make_float4(acc[4], acc[5], acc[6], acc[7]);
  }
  BAR();
  if (tid < 288) {
    float v0 = 0.f, v1 = 0.f;
#pragma unroll
    for (int r = 0; r < 8; ++r) { const float2 t = *(const float2*)&sm.red[r][2 * tid]; v0 += t.x; v1 += t.y; }
    u32x4 gv; gv.x = __float_as_uint(v0); gv.y = tag; gv.z = __float_as_uint(v1); gv.w = tag;
    st_sc1(rs, WG_OFF + ((tid >> 2) * 256 + c) * 64 + (tid & 3) * 16, gv);
  }
  CFENCE();
  CFENCE();
  SCHED();
  u32x4 w1v[4], w2v[4];
  unsigned short b1r[4];
  if (HAS_MLP) {
    const unsigned short* p1 = w1 + (size_t)(tid >> 2) * FF + c * 32 + (tid & 3) * 8;
    const unsigned short* p2 = w2 + (size_t)c * 32 * 576 + tid * 8;
#pragma unroll
    for (int i = 0; i < 4; ++i) w1v[i] = ld_nt(p1 + (size_t)i * 144 * FF);
#pragma unroll
    for (int i = 0; i < 4; ++i) w2v[i] = ld_nt(p2 + (size_t)i * 576 * 8);
#pragma unroll
    for (int i = 0; i < 4; ++i) b1r[i] = b1[c * 32 + tid / 72 + 8 * i];
  }
  CFENCE();
  SCHED();
  if (IS_RED) {
    const int r = c - NMLP;
    if (wave < 8) {
      int off[2];
      u32x4 a[2];
      off[0] = WG_OFF + (r * 256 + wave * 32 + (lane >> 1)) * 64 + (lane & 1) * 32;
      off[1] = off[0] + 16;
      gather<2>(rs, off, tag, a);
      float v[4] = {__uint_as_float(a[0].x), __uint_as_float(a[0].z), __uint_as_float(a[1].x), __uint_as_float(a[1].z)};
#pragma unroll
      for (int k = 0; k < 4; ++k) {
        float t = v[k];
        t += dppf<DPP_X2>(t); t += dppf<DPP_ROR4>(t); t += dppf<DPP_ROR8>(t);
        v[k] = sum_x32(sum_x16(t));
      }
      if (lane < 2) *(float4*)&sm.part[wave][lane * 4] = make_float4(v[0], v[1], v[2], v[3]);
    }
    BAR();
    if (tid < 4) {
      const unsigned xw = ((const unsigned*)x)[4 * r + tid];
      float v0 = 0.f, v1 = 0.f;
#pragma unroll
      for (int w = 0; w < 8; ++w) { v0 += sm.part[w][2 * tid]; v1 += sm.part[w][2 * tid + 1]; }
      u32x4 g0;
      g0.x = __float_as_uint(rbf(bf_lo(xw) + rbf(v0))); g0.y = tag;
      g0.z = __float_as_uint(rbf(bf_hi(xw) + rbf(v1))); g0.w = tag;
      st_sc1(rs, HG_OFF + (8 * r + 2 * tid) * 8, g0);
    }
    DSTAMP(6);
  }
  if (HAS_MLP) {
    if (wave < 5 && tid < 288) {
      int off[1];
      u32x4 a[1];
      off[0] = HG_OFF + tid * 16;
      gather<1>(rs, off, tag, a);
      *(float2*)&sm.h[2 * tid] = make_float2(__uint_as_float(a[0].x), __uint_as_float(a[0].z));
    }
    DSTAMP(7);
    CSTAMP(14);
    BAR();
    DSTAMP(12);
    {
      float hv[9];
      float sum = 0.f;
#pragma unroll
      for (int i = 0; i < 9; ++i) { hv[i] = sm.h[lane + 64 * i]; sum += hv[i]; }
      sum = sum_wave(sum);
      const float mean = sum * (1.f / 576.f);
      float sq = 0.f;
#pragma unroll
      for (int i = 0; i < 9; ++i) { const float d = hv[i] - mean; sq += d * d; }
      sq = sum_wave(sq);
      const float mb = rbf(mean);
      const float sd = rbf(sqrtf(rbf(rbf(sq * (1.f / 576.f)) + 1e-5f)));
      float acc[8];
#pragma unroll
      for (int k = 0; k < 8; ++k) acc[k] = 0.f;
#pragma unroll
      for (int i = 0; i < 4; ++i) fma8(acc, rbf(rbf(sm.h[(tid >> 2) + 144 * i] - mb) / sd), w1v[i]);
#pragma unroll
      for (int k = 0; k < 8; ++k) {
        float a = acc[k];
        a += dppf<DPP_ROR4>(a); a += dppf<DPP_ROR8>(a);
        acc[k] = sum_x32(sum_x16(a));
      }
      if (lane < 4) {
        *(float4*)&sm.w1red[wave][lane * 8] = make_float4(acc[0], acc[1], acc[2], acc[3]);
        *(float4*)&sm.w1red[wave][lane * 8 + 4] = make_float4(acc[4], acc[5], acc[6], acc[7]);
      }
    }
    BAR();
    DSTAMP(13);
    {
      const int r0 = tid / 72, cc = tid - r0 * 72;
      float acc[8];
#pragma unroll
      for (int k = 0; k < 8; ++k) acc[k] = 0.f;
#pragma unroll
      for (int i = 0; i < 4; ++i) {
        float v = 0.f;
#pragma unroll
        for (int w = 0; w < 9; ++w) v += sm.w1red[w][r0 + 8 * i];
        v = rbf(rbf(v) + bf_us(b1r[i]));
        v = fmaxf(v, 0.f);
        v = rbf(v * v);
        fma8(acc, v, w2v[i]);
      }
      *(float4*)&sm.red[r0][cc * 8] = make_float4(acc[0], acc[1], acc[2], acc[3]);
      *(float4*)&sm.red[r0][cc * 8 + 4] = make_float4(acc[4], acc[5], acc[6], acc[7]);
    }
    BAR();
    DSTAMP(8);
    CSTAMP(15);
    if (tid < 288) {
      float v0 = 0.f, v1 = 0.f;
#pragma unroll
      for (int r = 0; r < 8; ++r) { const float2 t = *(const float2*)&sm.red[r][2 * tid]; v0 += t.x; v1 += t.y; }
      u32x4 gv; gv.x = __float_as_uint(v0); gv.y = tag; gv.z = __float_as_uint(v1); gv.w = tag;
      st_sc1(rs, MG_OFF + ((tid >> 1) * 144 + c) * 32 + (tid & 1) * 16, gv);
    }
    if (wave < 5) {
      float v0 = 0.f, v1 = 0.f;
      if (tid < 288) {
        int off[1];
        u32x4 a[1];
        off[0] = MG_OFF + c * 144 * 32 + tid * 16;
        gather<1>(rs, off, tag, a);
        v0 = __uint_as_float(a[0].x); v1 = __uint_as_float(a[0].z);
      }
      v0 += dppf<DPP_X2>(v0); v0 += dppf<DPP_ROR4>(v0); v0 += dppf<DPP_ROR8>(v0); v0 = sum_x32(sum_x16(v0));
      v1 += dppf<DPP_X2>(v1); v1 += dppf<DPP_ROR4>(v1); v1 += dppf<DPP_ROR8>(v1); v1 = sum_x32(sum_x16(v1));
      if (lane < 2) *(float2*)&sm.part[wave][lane * 2] = make_float2(v0, v1);
    }
    BAR();
    if (tid < 2) {
      const unsigned bw = ((const unsigned*)b2)[2 * c + tid];
      float v0 = 0.f, v1 = 0.f;
#pragma unroll
      for (int w = 0; w < 5; ++w) { v0 += sm.part[w][2 * tid]; v1 += sm.part[w][2 * tid + 1]; }
      const float h0 = sm.h[4 * c + 2 * tid], h1 = sm.h[4 * c + 2 * tid + 1];
      ((unsigned*)out)[2 * c + tid] = (unsigned)to_bf_us(h0 + rbf(rbf(v0) + bf_lo(bw))) |
                                      ((unsigned)to_bf_us(h1 + rbf(rbf(v1) + bf_hi(bw))) << 16);
    }
    if (c == 0 && tid == 0) __builtin_amdgcn_raw_buffer_store_b32(tag, rs, EPOCH_OFF, 0, AUX_SC1);
    DSTAMP(9);
  }
}

}

__global__ void __launch_bounds__(640, 3)
fused_decode_block(const unsigned short* __restrict__ x, const unsigned short* __restrict__ kc,
                   const unsigned short* __restrict__ vc, const unsigned short* __restrict__ wq,
                   const unsigned short* __restrict__ wo, const unsigned short* __restrict__ w1,
                   const unsigned short* __restrict__ b1, const unsigned short* __restrict__ w2,
                   const unsigned short* __restrict__ b2, unsigned short* __restrict__ out, char* ws) {
  __shared__ __attribute__((aligned(16))) Smem sm;
  const int tid = threadIdx.x;
  const int lane = tid & 63;
  const int wave = __builtin_amdgcn_readfirstlane(tid >> 6);
  const int c = blockIdx.x;
  const int g = c >> 4;
  const __amdgpu_buffer_rsrc_t rs = __builtin_amdgcn_make_buffer_rsrc((void*)ws, 0, WS_BYTES, 0x00020000);

  if (wave < 9) {
    data_body(sm, tid, lane, wave, c, rs, kc, vc, wo, w1, b1, w2, x, b2, out, ws);
  } else {
    STAMP(0);
    const int s = c & 15, cs = s & 3, kq = s >> 2;
    const unsigned epoch = __builtin_amdgcn_raw_buffer_load_b32(rs, EPOCH_OFF, 0, AUX_SC1);
    unsigned xd[5];
#pragma unroll
    for (int i = 0; i < 5; ++i) {
      const int idx = lane + 64 * i;
      xd[i] = ((const unsigned*)x)[idx < 288 ? idx : 287];
    }
    u32x4 wqv[18];
    {
      const unsigned short* p = wq + (size_t)(kq * 144 + (lane >> 3)) * 4096 + g * 256 + cs * 64 + (lane & 7) * 8;
#pragma unroll
      for (int i = 0; i < 18; ++i) wqv[i] = ld_nt(p + (size_t)i * 8 * 4096);
    }
    CFENCE();
    SCHED();
    RAWBAR();
    SCHED();
    const unsigned tag = epoch + 1u;
    {
      float sum = 0.f;
#pragma unroll
      for (int i = 0; i < 5; ++i) sum += (lane + 64 * i < 288) ? (bf_lo(xd[i]) + bf_hi(xd[i])) : 0.f;
      sum = sum_wave(sum);
      const float mean = sum * (1.f / 576.f);
      float sq = 0.f;
#pragma unroll
      for (int i = 0; i < 5; ++i) {
        const float a = bf_lo(xd[i]) - mean, b = bf_hi(xd[i]) - mean;
        sq += (lane + 64 * i < 288) ? (a * a + b * b) : 0.f;
      }
      sq = sum_wave(sq);
      const float mb = rbf(mean);
      const float sd = rbf(sqrtf(rbf(rbf(sq * (1.f / 576.f)) + 1e-5f)));
#pragma unroll
      for (int i = 0; i < 5; ++i) {
        if (lane + 64 * i < 288) {
          *(float2*)&sm.xn[2 * (lane + 64 * i)] =
              make_float2(rbf(rbf(bf_lo(xd[i]) - mb) / sd), rbf(rbf(bf_hi(xd[i]) - mb) / sd));
        }
      }
    }
    asm volatile("s_waitcnt lgkmcnt(0)" ::: "memory");
    STAMP(11);
    {
      float acc[8];
#pragma unroll
      for (int k = 0; k < 8; ++k) acc[k] = 0.f;
#pragma unroll
      for (int i = 0; i < 18; ++i) fma8(acc, sm.xn[kq * 144 + i * 8 + (lane >> 3)], wqv[i]);
#pragma unroll
      for (int k = 0; k < 8; ++k) {
        float a = acc[k];
        a += dppf<DPP_ROR8>(a);
        acc[k] = sum_x32(sum_x16(a));
      }
      if (lane < 8) {
        const int off = QG_OFF + (kq * 4096 + g * 256 + cs * 64 + lane * 8) * 8;
#pragma unroll
        for (int k = 0; k < 4; ++k) {
          u32x4 gv; gv.x = __float_as_uint(acc[2 * k]); gv.y = tag; gv.z = __float_as_uint(acc[2 * k + 1]); gv.w = tag;
          st_sc1(rs, off + k * 16, gv);
        }
      }
    }
    STAMP(1);
    RAWBAR();
    {
      float q4[4];
      {
        int off[8];
        u32x4 r[8];
#pragma unroll
        for (int k = 0; k < 4; ++k) {
          off[2 * k] = QG_OFF + (k * 4096 + g * 256 + lane * 4) * 8;
          off[2 * k + 1] = off[2 * k] + 16;
        }
        gather<8>(rs, off, tag, r);
#pragma unroll
        for (int k = 0; k < 4; ++k) q4[k] = 0.f;
#pragma unroll
        for (int k = 0; k < 4; ++k) {
          q4[0] += __uint_as_float(r[2 * k].x); q4[1] += __uint_as_float(r[2 * k].z);
          q4[2] += __uint_as_float(r[2 * k + 1].x); q4[3] += __uint_as_float(r[2 * k + 1].z);
        }
      }
      sm.qbf[lane * 2] = (unsigned)to_bf_us(q4[0]) | ((unsigned)to_bf_us(q4[1]) << 16);
      sm.qbf[lane * 2 + 1] = (unsigned)to_bf_us(q4[2]) | ((unsigned)to_bf_us(q4[3]) << 16);
      if (lane == 0) sm.tag = tag;
    }
    STAMP(2);
    BAR();
    BAR();
    STAMP(3);
    {
      const int hblk = AG_OFF + (c >> 3) * 16 * 144 * 8;
      const int j16 = (c & 7) * 16;
      {
        int off[3];
        u32x4 r[3];
        off[0] = hblk + (lane & 15) * 144 * 8;
        off[1] = hblk + ((lane >> 3) * 144 + 16 + j16 + (lane & 7) * 2) * 8;
        off[2] = hblk + ((8 + (lane >> 3)) * 144 + 16 + j16 + (lane & 7) * 2) * 8;
        gather<3>(rs, off, tag, r);
        if (lane < 16) { sm.raw[256 + lane * 2] = __uint_as_float(r[0].x); sm.raw[257 + lane * 2] = __uint_as_float(r[0].z); }
        sm.raw[(lane >> 3) * 16 + (lane & 7) * 2] = __uint_as_float(r[1].x);
        sm.raw[(lane >> 3) * 16 + (lane & 7) * 2 + 1] = __uint_as_float(r[1].z);
        sm.raw[(8 + (lane >> 3)) * 16 + (lane & 7) * 2] = __uint_as_float(r[2].x);
        sm.raw[(8 + (lane >> 3)) * 16 + (lane & 7) * 2 + 1] = __uint_as_float(r[2].z);
      }
      asm volatile("s_waitcnt lgkmcnt(0)" ::: "memory");
      if (lane < 16) {
        float M = sm.raw[256];
#pragma unroll
        for (int sp = 1; sp < 16; ++sp) M = fmaxf(M, sm.raw[256 + sp * 2]);
        float L = 0.f, o = 0.f;
#pragma unroll
        for (int sp = 0; sp < 16; ++sp) {
          const float e = __expf(sm.raw[256 + sp * 2] - M);
          L += e * sm.raw[257 + sp * 2];
          o += e * sm.raw[sp * 16 + lane];
        }
        sm.o16[lane] = rbf(o / L);
      }
    }
    STAMP(4);
    BAR();
    BAR();
    STAMP(5);
    if (c < NMLP) {
      BAR();
      BAR();
      BAR();
      BAR();
    } else if (c < NMLP + 72) {
      BAR();
    }
  }
}

extern "C" void kernel_launch(void* const* d_in, const int* in_sizes, int n_in,
                              void* d_out, int out_size, void* d_ws, size_t ws_size,
                              hipStream_t stream) {
  (void)in_sizes; (void)n_in; (void)out_size; (void)ws_size;
  fused_decode_block<<<256, 640, 0, stream>>>(
      (const unsigned short*)d_in[0], (const unsigned short*)d_in[1], (const unsigned short*)d_in[2],
      (const unsigned short*)d_in[3], (const unsigned short*)d_in[4], (const unsigned short*)d_in[6],
      (const unsigned short*)d_in[7], (const unsigned short*)d_in[8], (const unsigned short*)d_in[9],
      (unsigned short*)d_out, (char*)d_ws);
}
```
